```python
import torch

def _softmax(x, axis=-1):
    return torch.softmax(x, dim=axis)
def _gelu(x):
    return torch.nn.functional.gelu(x, approximate="tanh")
def _silu(x):
    return torch.nn.functional.silu(x)
def _sigmoid(x):
    return torch.sigmoid(x)
def _layernorm(x, axis=-1, eps=1e-5):
    m = x.mean(dim=axis, keepdim=True)
    v = x.var(dim=axis, keepdim=True, unbiased=False)
    return (x - m) / torch.sqrt(v + eps)
def _scatter_add(tgt, idx, src, axis=0):
    return tgt.clone().index_add_(axis, idx, src)
def _segment_sum(src, seg, n):
    out = torch.zeros((n,) + src.shape[1:], dtype=src.dtype)
    return out.index_add_(0, seg, src)
def _pad(x, pw):
    flat = tuple(v for pair in reversed(pw) for v in pair)
    return torch.nn.functional.pad(x, flat)

kernel_name = 'concat_heads__linear_kernel_hedgehog__sliding_block__prompt'

def setup_inputs():

    _w = (lambda a: (lambda t: t.to(torch.bfloat16) if t.is_floating_point() else t)(torch.as_tensor(a)))
    def _A_setup():
        B, S, D, H = 1, 2048, 512, 32
        Dh = D // H
        return {
            "x": _w((lambda *s: torch.randn(*s, dtype=torch.bfloat16) * 0.1)(B, S, D)),
            "wqkv": _w((lambda *s: torch.randn(*s, dtype=torch.bfloat16) * 1.6)(D, 3 * D)),
            "w_phi": _w((lambda *s: torch.randn(*s, dtype=torch.bfloat16) * 1.6)(H, Dh, Dh) * (Dh ** -0.5)),
            "wo": _w((lambda *s: torch.randn(*s, dtype=torch.bfloat16) * 3.2)(D, D)),
        }
    _A_in = _A_setup()

    def _B_setup():
        B, S, D = 1, 2048, 512
        return {
            "x": _w((lambda *s: torch.randn(*s, dtype=torch.bfloat16) * 0.2)(B, S, D)),
            "wqkv": _w((lambda *s: torch.randn(*s, dtype=torch.bfloat16) * 0.4)(D, 3 * D)),
            "wo": _w((lambda *s: torch.randn(*s, dtype=torch.bfloat16) * 0.4)(D, D)),
        }
    _B_in = _B_setup()

    return {
        "A_x": _A_in["x"],
        "A_wqkv": _A_in["wqkv"],
        "A_w_phi": _A_in["w_phi"],
        "A_wo": _A_in["wo"],
        "B_x": _B_in["x"],
        "B_wqkv": _B_in["wqkv"],
        "B_wo": _B_in["wo"],
    }

def reference(A_x, A_wqkv, A_w_phi, A_wo, B_x, B_wqkv, B_wo):
    def _A_ref(x, wqkv, w_phi, wo):
        B, S, D = x.shape
        H = 32; Dh = D // H
        qkv = (lambda x, *d: x.permute(*d))((x @ wqkv).reshape(B, S, 3, H, Dh), 2, 0, 3, 1, 4)
        q, k, v = qkv[0], qkv[1], qkv[2]
        pq = torch.einsum("bhsd,hdf->bhsf", q, w_phi)
        pk = torch.einsum("bhsd,hdf->bhsf", k, w_phi)
        phi_q = torch.concatenate([_softmax(pq, axis=-1), _softmax(-pq, axis=-1)], axis=-1)
        phi_k = torch.concatenate([_softmax(pk, axis=-1), _softmax(-pk, axis=-1)], axis=-1)
        kv = torch.einsum("bhsf,bhsd->bhfd", phi_k, v)
        z  = torch.einsum("bhsf->bhf", phi_k)
        num = torch.einsum("bhqf,bhfd->bhqd", phi_q, kv)
        den = torch.einsum("bhqf,bhf->bhq", phi_q, z)[..., None] + 1e-6
        o = num / den
        return (lambda x, *d: x.permute(*d))(o, 0, 2, 1, 3).reshape(B, S, D) @ wo

    def _B_ref(x, wqkv, wo):
        B, S, D = x.shape
        H = 32; Dh = D // H; W = 256
        qkv = (lambda x, *d: x.permute(*d))((x @ wqkv).reshape(B, S, 3, H, Dh), 2, 0, 3, 1, 4)
        q, k, v = qkv[0], qkv[1], qkv[2]
        scores = torch.einsum("bhqd,bhkd->bhqk", q, k) * (Dh ** -0.5)
        idx = torch.arange(S)
        mask = (idx[:, None] - idx[None, :] >= 0) & (idx[:, None] - idx[None, :] < W)
        scores = torch.where(mask[None, None], scores, -1e9)
        p = _softmax(scores, axis=-1)
        o = torch.einsum("bhqk,bhkd->bhqd", p, v)
        return (lambda x, *d: x.permute(*d))(o, 0, 2, 1, 3).reshape(B, S, D) @ wo

    a = _A_ref(A_x, A_wqkv, A_w_phi, A_wo)
    b = _B_ref(B_x, B_wqkv, B_wo)
    return torch.concatenate([a, b], axis=-1)
```

```cpp
#include <hip/hip_runtime.h>
#include <hip/hip_bf16.h>
#include <stdint.h>

typedef __bf16 bf16x8 __attribute__((ext_vector_type(8)));
typedef __bf16 bf16x4 __attribute__((ext_vector_type(4)));
typedef short s16x4 __attribute__((ext_vector_type(4)));
typedef short s16x8 __attribute__((ext_vector_type(8)));
typedef float f32x16 __attribute__((ext_vector_type(16)));
typedef float f32x4 __attribute__((ext_vector_type(4)));
typedef unsigned u32x4 __attribute__((ext_vector_type(4)));
typedef unsigned u32x2 __attribute__((ext_vector_type(2)));
typedef unsigned short u16;

#define LDS_AS __attribute__((address_space(3)))
#define GLB_AS __attribute__((address_space(1)))
#define DEVI __device__ __forceinline__
#define MFMA32(a, b, c) __builtin_amdgcn_mfma_f32_32x32x16_bf16((a), (b), (c), 0, 0, 0)

constexpr int SEQ = 2048, DM = 512, NH = 32, NT = 64;
constexpr float L2E = 1.4426950408889634f;

constexpr size_t FRAG_BYTES = (size_t)NH * NT * 1024;
constexpr size_t WS_QB = 0;
constexpr size_t WS_KB = WS_QB + FRAG_BYTES;
constexpr size_t WS_VB = WS_KB + FRAG_BYTES;
constexpr size_t WS_QA = WS_VB + FRAG_BYTES;
constexpr size_t WS_O = WS_QA + FRAG_BYTES;
constexpr size_t WS_KVP = WS_O + 2 * FRAG_BYTES;
constexpr int KVP_FLOATS = 544;

DEVI void glds16(const void* g, char* lds_base) {
  __builtin_amdgcn_global_load_lds((const GLB_AS void*)g, (LDS_AS void*)lds_base, 16, 0, 0);
}
DEVI void glds16s(const void* sbase, uint32_t voff, uint32_t lds_addr) {
  asm volatile("s_mov_b32 m0, %2\n\ts_nop 0\n\tglobal_load_lds_dwordx4 %0, %1" ::"v"(voff), "s"(sbase), "s"(lds_addr) : "memory");
}
DEVI uint32_t lds_addr_of(const char* p) { return (uint32_t)(uintptr_t)(const LDS_AS char*)p; }
DEVI bf16x8 lds_read16(const char* p) { return *(const bf16x8*)p; }
DEVI bf16x8 tr_frag(const char* p0, const char* p1) {
  s16x4 a = __builtin_amdgcn_ds_read_tr16_b64_v4i16((LDS_AS s16x4*)p0);
  s16x4 b = __builtin_amdgcn_ds_read_tr16_b64_v4i16((LDS_AS s16x4*)p1);
  s16x8 r = __builtin_shufflevector(a, b, 0, 1, 2, 3, 4, 5, 6, 7);
  return __builtin_bit_cast(bf16x8, r);
}
DEVI float swap_max(float x) {
  auto t = __builtin_amdgcn_permlane32_swap(__float_as_uint(x), __float_as_uint(x), false, false);
  return fmaxf(__uint_as_float(t[0]), __uint_as_float(t[1]));
}
DEVI float swap_add(float x) {
  auto t = __builtin_amdgcn_permlane32_swap(__float_as_uint(x), __float_as_uint(x), false, false);
  return __uint_as_float(t[0]) + __uint_as_float(t[1]);
}
template <int O>
DEVI bf16x8 pack8(const f32x16& a) {
  bf16x8 r;
#pragma unroll
  for (int j = 0; j < 8; ++j) r[j] = (__bf16)a[O + j];
  return r;
}
template <int O>
DEVI bf16x8 pack8s(const f32x16& a, float s) {
  bf16x8 r;
#pragma unroll
  for (int j = 0; j < 8; ++j) r[j] = (__bf16)(a[O + j] * s);
  return r;
}
DEVI f32x16 zero16() {
  f32x16 z;
#pragma unroll
  for (int i = 0; i < 16; ++i) z[i] = 0.f;
  return z;
}
DEVI bf16x8 load_wphi_frag(const u16* wphi, int head, int lane) {
  const int f = lane & 15, h = lane >> 5;
  const u16 sgn = (lane & 16) ? 0x8000 : 0;
  s16x8 r;
#pragma unroll
  for (int j = 0; j < 8; ++j) {
    const int d = 8 * (j >> 2) + 4 * h + (j & 3);
    r[j] = (short)(wphi[head * 256 + d * 16 + f] ^ sgn);
  }
  return __builtin_bit_cast(bf16x8, r);
}
DEVI bf16x8 wphi_frag_lds(const char* wl, int lane) {
  const char* p = wl + (4 * (lane >> 5) + ((lane & 15) >> 2)) * 32 + (lane & 3) * 8;
  const u32x4 r = __builtin_bit_cast(u32x4, tr_frag(p, p + 256));
  const unsigned sgn = (lane & 16) ? 0x80008000u : 0u;
  const u32x4 o = {r[0] ^ sgn, r[1] ^ sgn, r[2] ^ sgn, r[3] ^ sgn};
  return __builtin_bit_cast(bf16x8, o);
}
DEVI void dual_softmax(const f32x16& p, bf16x8& fp, bf16x8& fn) {
  float mp = fmaxf(fmaxf(fmaxf(p[0], p[1]), fmaxf(p[2], p[3])), fmaxf(fmaxf(p[4], p[5]), fmaxf(p[6], p[7])));
  float mn = fmaxf(fmaxf(fmaxf(p[8], p[9]), fmaxf(p[10], p[11])), fmaxf(fmaxf(p[12], p[13]), fmaxf(p[14], p[15])));
  mp = swap_max(mp) * L2E;
  mn = swap_max(mn) * L2E;
  f32x16 e;
  float sp = 0.f, sn = 0.f;
#pragma unroll
  for (int j = 0; j < 8; ++j) {
    e[j] = __builtin_amdgcn_exp2f(__builtin_fmaf(p[j], L2E, -mp));
    e[8 + j] = __builtin_amdgcn_exp2f(__builtin_fmaf(p[8 + j], L2E, -mn));
    sp += e[j];
    sn += e[8 + j];
  }
  sp = __builtin_amdgcn_rcpf(swap_add(sp));
  sn = __builtin_amdgcn_rcpf(swap_add(sn));
  fp = pack8s<0>(e, sp);
  fn = pack8s<8>(e, sn);
}

constexpr int STAGE_BYTES = 40960;
constexpr int K1_WPHI = 3 * STAGE_BYTES;
__global__ __launch_bounds__(512, 2) void k1_qkv(const u16* __restrict__ xA, const u16* __restrict__ wA,
                                                 const u16* __restrict__ wphi, const u16* __restrict__ xB,
                                                 const u16* __restrict__ wB, char* __restrict__ ws) {
  __shared__ __attribute__((aligned(16))) char smem[3 * STAGE_BYTES + 2048];
  const int tid = threadIdx.x, lane = tid & 63;
  const int w = __builtin_amdgcn_readfirstlane(tid >> 6);
  const int bid = blockIdx.x, xcd = bid & 7, idx = bid >> 3;
  const int br = xcd >> 2, jx = xcd & 3;
  const int ti = (jx & 1) * 8 + (idx & 7);
  const int g = (jx >> 1) * 4 + (idx >> 3);
  const u16* x = br ? xB : xA;
  const u16* W = br ? wB : wA;
  const int tt = w & 3, hp = w >> 2;
  const int h = lane >> 5;

  const uint32_t sm0 = lds_addr_of(smem);
  const uint32_t xo0 = (uint32_t)((ti * 128 + 16 * w + (lane >> 3)) * DM + (((lane & 7) ^ ((lane >> 4) & 7)) * 8)) * 2u;
  const uint32_t xo1 = (uint32_t)((ti * 128 + 16 * w + 8 + (lane >> 3)) * DM + (((lane & 7) ^ ((4 + (lane >> 4)) & 7)) * 8)) * 2u;
  const uint32_t wo0 = (uint32_t)((8 * w + (lane >> 3)) * 1536 + g * 64 + (((lane & 7) ^ (((lane >> 4) & 1) << 2)) * 8)) * 2u;
  auto stage_load = [&](int s, int buf) {
    const uint32_t b = sm0 + buf * STAGE_BYTES;
    glds16s(x + s * 64, xo0, b + w * 2048);
    glds16s(x + s * 64, xo1, b + w * 2048 + 1024);
    glds16s(W + (size_t)s * 64 * 1536, wo0, b + 16384 + w * 1024);
    glds16s(W + (size_t)s * 64 * 1536 + 512, wo0, b + 16384 + 8192 + w * 1024);
    glds16s(W + (size_t)s * 64 * 1536 + 1024, wo0, b + 16384 + 16384 + w * 1024);
  };
  if (br == 0 && w < 2) glds16s(wphi + (size_t)(4 * g) * 256 + w * 512, (uint32_t)lane * 16u, sm0 + K1_WPHI + w * 1024);
  stage_load(0, 0);
  stage_load(1, 1);

  const int xrow = 32 * tt + (lane & 31);
  const int xa0 = xrow * 128 + ((h ^ ((xrow >> 1) & 7)) * 16);
  const int blk = (lane >> 4) & 1, q4 = (lane & 15) >> 2, p4 = lane & 3;
  const int wa0 = 16384 + (8 * h + q4) * 128 + (((4 * hp + 2 * blk + (p4 >> 1)) ^ (((q4 >> 1) & 1) << 2)) * 16) + 8 * (p4 & 1);

  f32x16 aq = zero16(), ak = zero16(), av = zero16();
#pragma unroll
  for (int s = 0; s < 8; ++s) {
    if (s < 7) asm volatile("s_waitcnt vmcnt(5)" ::: "memory");
    else asm volatile("s_waitcnt vmcnt(0)" ::: "memory");
    __builtin_amdgcn_s_barrier();
    asm volatile("" ::: "memory");
    if (s + 2 < 8) stage_load(s + 2, (s + 2) % 3);
    const char* b = smem + (s % 3) * STAGE_BYTES;
#pragma unroll
    for (int ks = 0; ks < 4; ++ks) {
      bf16x8 xf = lds_read16(b + (xa0 ^ (32 * ks)));
      const char* wb = b + wa0 + ks * 2048;
      bf16x8 fq = tr_frag(wb, wb + 512);
      bf16x8 fk = tr_frag(wb + 8192, wb + 8192 + 512);
      bf16x8 fv = tr_frag(wb + 16384, wb + 16384 + 512);
      aq = MFMA32(fq, xf, aq);
      ak = MFMA32(fk, xf, ak);
      av = MFMA32(xf, fv, av);
    }
    asm volatile("s_waitcnt lgkmcnt(0)" ::: "memory");
  }

  const int tile32 = ti * 4 + tt;
  const int head0 = 4 * g + 2 * hp;
  if (br == 1) {
    constexpr float QS = 0.25f * L2E;
    char* qb = ws + WS_QB + ((size_t)head0 * NT + tile32) * 1024 + lane * 16;
    *(bf16x8*)qb = pack8s<0>(aq, QS);
    *(bf16x8*)(qb + NT * 1024) = pack8s<8>(aq, QS);
    char* kb = ws + WS_KB + ((size_t)head0 * NT + tile32) * 1024 + lane * 16;
    *(bf16x8*)kb = pack8<0>(ak);
    *(bf16x8*)(kb + NT * 1024) = pack8<8>(ak);
    char* vb = ws + WS_VB + ((size_t)(head0 + ((lane >> 4) & 1)) * NT + tile32) * 1024 + h * 256 + (lane & 15) * 16;
    *(bf16x8*)vb = pack8<0>(av);
    *(bf16x8*)(vb + 512) = pack8<8>(av);
    return;
  }

  char* qa = ws + WS_QA + ((size_t)head0 * NT + tile32) * 1024 + lane * 16;
  *(bf16x8*)qa = pack8<0>(aq);
  *(bf16x8*)(qa + NT * 1024) = pack8<8>(aq);

  s16x8 i0, i1;
#pragma unroll
  for (int j = 0; j < 8; ++j) {
    const int f = 8 * (j >> 2) + 4 * h + (j & 3);
    i0[j] = ((lane & 31) == f) ? (short)0x3F80 : (short)0;
    i1[j] = ((lane & 31) == 16 + f) ? (short)0x3F80 : (short)0;
  }
  const bf16x8 id0 = __builtin_bit_cast(bf16x8, i0), id1 = __builtin_bit_cast(bf16x8, i1);
  const bf16x8 vf0 = pack8<0>(av), vf1 = pack8<8>(av);

  __builtin_amdgcn_s_barrier();
  float* red = (float*)smem;
  float* zred = (float*)(smem + 32768);
#pragma unroll
  for (int p = 0; p < 2; ++p) {
    const bf16x8 kf = p ? pack8<8>(ak) : pack8<0>(ak);
    f32x16 pk = MFMA32(wphi_frag_lds(smem + K1_WPHI + (2 * hp + p) * 512, lane), kf, zero16());
    bf16x8 fp, fn;
    dual_softmax(pk, fp, fn);
    f32x16 T = MFMA32(fp, id0, zero16());
    T = MFMA32(fn, id1, T);
    float z = 0.f;
#pragma unroll
    for (int r = 0; r < 16; ++r) z += T[r];
    z = swap_add(z);
    f32x16 kv = MFMA32(pack8<0>(T), vf0, zero16());
    kv = MFMA32(pack8<8>(T), vf1, kv);
    const int hl = 2 * hp + p;
    if (((lane >> 4) & 1) == p) {
      float* dst = red + (size_t)(tt * 4 + hl) * 512 + (h * 16 + (lane & 15)) * 4;
#pragma unroll
      for (int r4 = 0; r4 < 4; ++r4) {
        f32x4 v = {kv[4 * r4], kv[4 * r4 + 1], kv[4 * r4 + 2], kv[4 * r4 + 3]};
        *(f32x4*)(dst + r4 * 128) = v;
      }
    }
    if (lane < 32) zred[(tt * 4 + hl) * 32 + lane] = z;
  }
  __syncthreads();
  {
    const int hl = tid >> 7, pos = tid & 127;
    f32x4 s = *(const f32x4*)(red + (0 * 4 + hl) * 512 + pos * 4);
#pragma unroll
    for (int t = 1; t < 4; ++t) s += *(const f32x4*)(red + (t * 4 + hl) * 512 + pos * 4);
    float* dst = (float*)(ws + WS_KVP) + ((size_t)(4 * g + hl) * 16 + ti) * KVP_FLOATS;
    *(f32x4*)(dst + pos * 4) = s;
    if (tid < 128) {
      const int hz = tid >> 5, f = tid & 31;
      const float z = (zred[(0 * 4 + hz) * 32 + f] + zred[(1 * 4 + hz) * 32 + f]) +
                      (zred[(2 * 4 + hz) * 32 + f] + zred[(3 * 4 + hz) * 32 + f]);
      ((float*)(ws + WS_KVP))[((size_t)(4 * g + hz) * 16 + ti) * KVP_FLOATS + 512 + f] = z;
    }
  }
}

constexpr int K2_K = 0, K2_V = 16384, K2_ONES = 32768, K2_RED = 33792, K2_ZRED = K2_RED + 8192, K2_KVF = K2_ZRED + 256;
constexpr int K2_LDS = K2_KVF + 2048;
__global__ __launch_bounds__(512, 2) void k2_attn(const u16* __restrict__ wphi, char* __restrict__ ws) {
  __shared__ __attribute__((aligned(16))) char smem[K2_LDS];
  const int tid = threadIdx.x, lane = tid & 63;
  const int w = __builtin_amdgcn_readfirstlane(tid >> 6);
  const int head = blockIdx.x & 31, chunk = blockIdx.x >> 5;
  const int qt = chunk * 8 + w;
  const int h = lane >> 5;

  const bf16x8 qf = *(const bf16x8*)(ws + WS_QB + ((size_t)head * NT + qt) * 1024 + lane * 16);
  const bf16x8 qa = *(const bf16x8*)(ws + WS_QA + ((size_t)head * NT + qt) * 1024 + lane * 16);
  const bf16x8 wf = load_wphi_frag(wphi, head, lane);
  {
    const float* kvp = (const float*)(ws + WS_KVP) + (size_t)head * 16 * KVP_FLOATS;
    const int grp = tid >> 7, pos = tid & 127;
    f32x4 s0 = *(const f32x4*)(kvp + (grp * 4 + 0) * KVP_FLOATS + pos * 4);
    f32x4 s1 = *(const f32x4*)(kvp + (grp * 4 + 1) * KVP_FLOATS + pos * 4);
    f32x4 s2 = *(const f32x4*)(kvp + (grp * 4 + 2) * KVP_FLOATS + pos * 4);
    f32x4 s3 = *(const f32x4*)(kvp + (grp * 4 + 3) * KVP_FLOATS + pos * 4);
    f32x4 zz = {0.f, 0.f, 0.f, 0.f};
    if (tid < 128) zz = *(const f32x4*)(kvp + (tid >> 3) * KVP_FLOATS + 512 + (tid & 7) * 4);
    const int t0 = chunk * 8 - 8;
#pragma unroll
    for (int i = 0; i < 2; ++i) {
      const int lt = 2 * w + i;
      if (t0 + lt >= 0) {
        glds16(ws + WS_KB + ((size_t)head * NT + t0 + lt) * 1024 + lane * 16, smem + K2_K + lt * 1024);
        glds16(ws + WS_VB + ((size_t)head * NT + t0 + lt) * 1024 + lane * 16, smem + K2_V + lt * 1024);
      }
    }
    if (tid < 64) {
      u32x4 ones = {0x3F803F80u, 0x3F803F80u, 0x3F803F80u, 0x3F803F80u};
      *(u32x4*)(smem + K2_ONES + tid * 16) = ones;
    }
    *(f32x4*)(smem + K2_RED + grp * 2048 + pos * 16) = (s0 + s1) + (s2 + s3);
    if (tid < 128) {
#pragma unroll
      for (int c = 0; c < 4; ++c) {
        float v = zz[c];
        v += __shfl_xor(v, 8);
        v += __shfl_xor(v, 16);
        v += __shfl_xor(v, 32);
        zz[c] = v;
      }
      if (lane < 8) *(f32x4*)(smem + K2_ZRED + w * 128 + lane * 16) = zz;
    }
  }
  asm volatile("s_waitcnt vmcnt(0)" ::: "memory");
  __syncthreads();
  if (tid < 128) {
    const int pos = tid, r4 = pos >> 5, hd = pos & 31;
    f32x4 s = *(const f32x4*)(smem + K2_RED + pos * 16);
#pragma unroll
    for (int gq = 1; gq < 4; ++gq) s += *(const f32x4*)(smem + K2_RED + gq * 2048 + pos * 16);
    bf16x4 o;
#pragma unroll
    for (int c = 0; c < 4; ++c) o[c] = (__bf16)s[c];
    *(bf16x4*)(smem + K2_KVF + (r4 >> 1) * 1024 + ((hd >> 4) * 32 + (hd & 15)) * 16 + (r4 & 1) * 8) = o;
  }
  {
    const int f = tid & 31, dup = tid >> 5;
    const float* zr = (const float*)(smem + K2_ZRED);
    const float z = zr[f] + zr[32 + f];
    *(__bf16*)(smem + K2_KVF + (f >> 4) * 1024 + (((f >> 2) & 1) * 32 + 16 + dup) * 16 + (((f >> 3) & 1) * 4 + (f & 3)) * 2) = (__bf16)z;
  }

  {
    const char* kbase = smem + K2_K + w * 1024 + lane * 16;
    const bool lower = (lane & 16) == 0;
    const int vinc = lower ? 1024 : 0;
    const char* vbase = lower ? (smem + K2_V + w * 1024 + h * 256 + (lane & 15) * 16) : (smem + K2_ONES + (lane & 15) * 16);
    const int ql = lane & 31;
    const bool has0 = qt >= 8;
    f32x16 sm = MFMA32(lds_read16(kbase + 8 * 1024), qf, zero16());
    {
      f32x16 s0;
      if (has0) s0 = MFMA32(lds_read16(kbase), qf, zero16());
      else {
#pragma unroll
        for (int r = 0; r < 16; ++r) s0[r] = -INFINITY;
      }
#pragma unroll
      for (int r = 0; r < 16; ++r) {
        const int kl = (r & 3) + 8 * (r >> 2) + 4 * h;
        sm[r] = (kl <= ql) ? sm[r] : s0[r];
      }
    }
    float m = sm[0];
#pragma unroll
    for (int r = 1; r < 16; ++r) m = fmaxf(m, sm[r]);
#pragma unroll
    for (int i = 1; i < 8; ++i) {
      if (qt - 8 + i >= 0) {
        f32x16 s = MFMA32(lds_read16(kbase + i * 1024), qf, zero16());
#pragma unroll
        for (int r = 0; r < 16; r += 2) m = fmaxf(fmaxf(m, s[r]), s[r + 1]);
      }
    }
    m = swap_max(m);
    f32x16 negm;
#pragma unroll
    for (int r = 0; r < 16; ++r) negm[r] = -m;
    f32x16 o = zero16();
    {
      f32x16 e;
#pragma unroll
      for (int r = 0; r < 16; ++r) e[r] = __builtin_amdgcn_exp2f(sm[r] - m);
      const u32x4 pa = __builtin_bit_cast(u32x4, pack8<0>(e)), pb = __builtin_bit_cast(u32x4, pack8<8>(e));
      u32x4 da, db, ua, ub;
#pragma unroll
      for (int i = 0; i < 4; ++i) {
        const int ra = 2 * i, rb = 8 + 2 * i;
        const int ka = (ra & 3) + 8 * (ra >> 2) + 4 * h, kb = (rb & 3) + 8 * (rb >> 2) + 4 * h;
        const unsigned ma = (ka <= ql ? 0xFFFFu : 0u) | (ka + 1 <= ql ? 0xFFFF0000u : 0u);
        const unsigned mb = (kb <= ql ? 0xFFFFu : 0u) | (kb + 1 <= ql ? 0xFFFF0000u : 0u);
        da[i] = pa[i] & ma; ua[i] = pa[i] & ~ma;
        db[i] = pb[i] & mb; ub[i] = pb[i] & ~mb;
      }
      const char* vd = vbase + 8 * vinc;
      o = MFMA32(lds_read16(vd), __builtin_bit_cast(bf16x8, da), o);
      o = MFMA32(lds_read16(vd + 512), __builtin_bit_cast(bf16x8, db), o);
      if (has0) {
        o = MFMA32(lds_read16(vbase), __builtin_bit_cast(bf16x8, ua), o);
        o = MFMA32(lds_read16(vbase + 512), __builtin_bit_cast(bf16x8, ub), o);
      }
    }
#pragma unroll
    for (int i = 1; i < 8; ++i) {
      if (qt - 8 + i >= 0) {
        f32x16 s = MFMA32(lds_read16(kbase + i * 1024), qf, negm);
        f32x16 e;
#pragma unroll
        for (int r = 0; r < 16; ++r) e[r] = __builtin_amdgcn_exp2f(s[r]);
        const char* vp = vbase + i * vinc;
        o = MFMA32(lds_read16(vp), pack8<0>(e), o);
        o = MFMA32(lds_read16(vp + 512), pack8<8>(e), o);
      }
    }
    const float inv = __builtin_amdgcn_rcpf(o[8]);
    *(bf16x8*)(ws + WS_O + FRAG_BYTES + ((size_t)head * NT + qt) * 1024 + lane * 16) = pack8s<0>(o, inv);
  }

  __syncthreads();
  {
    f32x16 pq = MFMA32(wf, qa, zero16());
    bf16x8 fp, fn;
    dual_softmax(pq, fp, fn);
    f32x16 o = MFMA32(lds_read16(smem + K2_KVF + lane * 16), fp, zero16());
    o = MFMA32(lds_read16(smem + K2_KVF + 1024 + lane * 16), fn, o);
    const float inv = __builtin_amdgcn_rcpf(o[8] + 1e-6f);
    *(bf16x8*)(ws + WS_O + ((size_t)head * NT + qt) * 1024 + lane * 16) = pack8s<0>(o, inv);
  }
}

constexpr int K3_LDS = 65536;
__global__ __launch_bounds__(512, 2) void k3_out(const u16* __restrict__ woA, const u16* __restrict__ woB,
                                                 const char* __restrict__ ws, u16* __restrict__ out) {
  __shared__ __attribute__((aligned(16))) char smem[K3_LDS];
  const int tid = threadIdx.x, lane = tid & 63;
  const int w = __builtin_amdgcn_readfirstlane(tid >> 6);
  const int xcd = blockIdx.x & 7, idx = blockIdx.x >> 3;
  const int br = xcd >> 2, ti = (xcd & 3) * 4 + (idx & 3), nj = idx >> 2;
  const u16* wo = br ? woB : woA;
  const int tt = w & 3, kh = w >> 2, h = lane >> 5;
  const int tile32 = ti * 4 + tt;

  const u16* wg = wo + (size_t)(256 * kh + 16 * tt + (lane >> 3)) * DM + nj * 64 + (((lane & 7) ^ (((lane >> 4) & 1) << 2)) * 8);
  const char* og = ws + WS_O + (size_t)br * FRAG_BYTES + ((size_t)(16 * kh) * NT + tile32) * 1024 + lane * 16;
  u32x4 wr[4][2];
  bf16x8 of[4][4];
#pragma unroll
  for (int c = 0; c < 4; ++c) {
    wr[c][0] = *(const u32x4*)(wg + (size_t)(64 * c) * DM);
    wr[c][1] = *(const u32x4*)(wg + (size_t)(64 * c + 8) * DM);
#pragma unroll
    for (int i = 0; i < 4; ++i) of[c][i] = *(const bf16x8*)(og + (size_t)(4 * c + i) * NT * 1024);
  }
  const int blk = (lane >> 4) & 1, q4 = (lane & 15) >> 2, p4 = lane & 3;
  f32x16 acc0 = zero16(), acc1 = zero16();
  char* wdst = smem + (256 * kh + 16 * tt) * 128 + lane * 16;
  const int ra0 = (256 * kh + 4 * h + q4) * 128 + (((2 * blk + (p4 >> 1)) ^ (((q4 >> 1) & 1) << 2)) * 16) + 8 * (p4 & 1);
  const int ra1 = (256 * kh + 4 * h + q4) * 128 + (((4 + 2 * blk + (p4 >> 1)) ^ (((q4 >> 1) & 1) << 2)) * 16) + 8 * (p4 & 1);
#pragma unroll
  for (int c = 0; c < 4; ++c) {
    *(u32x4*)(wdst + c * 64 * 128) = wr[c][0];
    *(u32x4*)(wdst + c * 64 * 128 + 1024) = wr[c][1];
    __syncthreads();
#pragma unroll
    for (int i = 0; i < 4; ++i) {
      const int kb = (64 * c + 16 * i) * 128;
      const bf16x8 a0 = tr_frag(smem + ra0 + kb, smem + ra0 + kb + 1024);
      const bf16x8 a1 = tr_frag(smem + ra1 + kb, smem + ra1 + kb + 1024);
      acc0 = MFMA32(a0, of[c][i], acc0);
      acc1 = MFMA32(a1, of[c][i], acc1);
    }
  }
  __syncthreads();
  float* ex = (float*)smem + w * 1024 + lane * 4;
  const f32x16 give = kh ? acc0 : acc1;
  f32x16 keep = kh ? acc1 : acc0;
#pragma unroll
  for (int r4 = 0; r4 < 4; ++r4) {
    f32x4 v = {give[4 * r4], give[4 * r4 + 1], give[4 * r4 + 2], give[4 * r4 + 3]};
    *(f32x4*)(ex + r4 * 256) = v;
  }
  __syncthreads();
  const float* px = (const float*)smem + (w ^ 4) * 1024 + lane * 4;
#pragma unroll
  for (int r4 = 0; r4 < 4; ++r4) {
    const f32x4 v = *(const f32x4*)(px + r4 * 256);
#pragma unroll
    for (int c = 0; c < 4; ++c) keep[4 * r4 + c] += v[c];
  }
  const u32x4 lo = __builtin_bit_cast(u32x4, pack8<0>(keep)), hi = __builtin_bit_cast(u32x4, pack8<8>(keep));
  u16* op = out + (size_t)(tile32 * 32 + (lane & 31)) * 1024 + br * 512 + nj * 64 + kh * 32 + h * 8;
  {
    auto t0 = __builtin_amdgcn_permlane32_swap(lo[0], lo[2], false, false);
    auto t1 = __builtin_amdgcn_permlane32_swap(lo[1], lo[3], false, false);
    u32x4 v = {t0[0], t1[0], t0[1], t1[1]};
    *(u32x4*)op = v;
    auto t2 = __builtin_amdgcn_permlane32_swap(hi[0], hi[2], false, false);
    auto t3 = __builtin_amdgcn_permlane32_swap(hi[1], hi[3], false, false);
    u32x4 u = {t2[0], t3[0], t2[1], t3[1]};
    *(u32x4*)(op + 16) = u;
  }
}

extern "C" void kernel_launch(void* const* d_in, const int* in_sizes, int n_in, void* d_out, int out_size,
                              void* d_ws, size_t ws_size, hipStream_t stream) {
  const u16* xA = (const u16*)d_in[0];
  const u16* wA = (const u16*)d_in[1];
  const u16* wphi = (const u16*)d_in[2];
  const u16* woA = (const u16*)d_in[3];
  const u16* xB = (const u16*)d_in[4];
  const u16* wB = (const u16*)d_in[5];
  const u16* woB = (const u16*)d_in[6];
  char* ws = (char*)d_ws;
  k1_qkv<<<256, 512, 0, stream>>>(xA, wA, wphi, xB, wB, ws);
  k2_attn<<<256, 512, 0, stream>>>(wphi, ws);
  k3_out<<<256, 512, 0, stream>>>(woA, woB, ws, (u16*)d_out);
}
```

```cpp
#include <hip/hip_runtime.h>
#include <hip/hip_bf16.h>
#include <stdint.h>

typedef __bf16 bf16x8 __attribute__((ext_vector_type(8)));
typedef __bf16 bf16x4 __attribute__((ext_vector_type(4)));
typedef short s16x4 __attribute__((ext_vector_type(4)));
typedef short s16x8 __attribute__((ext_vector_type(8)));
typedef float f32x16 __attribute__((ext_vector_type(16)));
typedef float f32x4 __attribute__((ext_vector_type(4)));
typedef unsigned u32x4 __attribute__((ext_vector_type(4)));
typedef unsigned u32x2 __attribute__((ext_vector_type(2)));
typedef unsigned short u16;

#define LDS_AS __attribute__((address_space(3)))
#define GLB_AS __attribute__((address_space(1)))
#define DEVI __device__ __forceinline__
#define MFMA32(a, b, c) __builtin_amdgcn_mfma_f32_32x32x16_bf16((a), (b), (c), 0, 0, 0)

#define STAMP(kid, i)
constexpr int SEQ = 2048, DM = 512, NH = 32, NT = 64;
constexpr float L2E = 1.4426950408889634f;

constexpr size_t FRAG_BYTES = (size_t)NH * NT * 1024;
constexpr size_t WS_QB = 0;
constexpr size_t WS_KB = WS_QB + FRAG_BYTES;
constexpr size_t WS_VB = WS_KB + FRAG_BYTES;
constexpr size_t WS_QA = WS_VB + FRAG_BYTES;
constexpr size_t WS_O = WS_QA + FRAG_BYTES;
constexpr size_t WS_KVP = WS_O + 2 * FRAG_BYTES;
constexpr int KVP_FLOATS = 544;

DEVI void glds16(const void* g, char* lds_base) {
  __builtin_amdgcn_global_load_lds((const GLB_AS void*)g, (LDS_AS void*)lds_base, 16, 0, 0);
}
DEVI void glds16s(const void* sbase, uint32_t voff, uint32_t lds_addr) {
  asm volatile("s_mov_b32 m0, %2\n\ts_nop 0\n\tglobal_load_lds_dwordx4 %0, %1" ::"v"(voff), "s"(sbase), "s"(lds_addr) : "memory");
}
DEVI uint32_t lds_addr_of(const char* p) { return (uint32_t)(uintptr_t)(const LDS_AS char*)p; }
DEVI bf16x8 lds_read16(const char* p) { return *(const bf16x8*)p; }
DEVI bf16x8 tr_frag(const char* p0, const char* p1) {
  s16x4 a = __builtin_amdgcn_ds_read_tr16_b64_v4i16((LDS_AS s16x4*)p0);
  s16x4 b = __builtin_amdgcn_ds_read_tr16_b64_v4i16((LDS_AS s16x4*)p1);
  s16x8 r = __builtin_shufflevector(a, b, 0, 1, 2, 3, 4, 5, 6, 7);
  return __builtin_bit_cast(bf16x8, r);
}
DEVI float swap_max(float x) {
  auto t = __builtin_amdgcn_permlane32_swap(__float_as_uint(x), __float_as_uint(x), false, false);
  return fmaxf(__uint_as_float(t[0]), __uint_as_float(t[1]));
}
DEVI float swap_add(float x) {
  auto t = __builtin_amdgcn_permlane32_swap(__float_as_uint(x), __float_as_uint(x), false, false);
  return __uint_as_float(t[0]) + __uint_as_float(t[1]);
}
template <int O>
DEVI bf16x8 pack8(const f32x16& a) {
  bf16x8 r;
#pragma unroll
  for (int j = 0; j < 8; ++j) r[j] = (__bf16)a[O + j];
  return r;
}
template <int O>
DEVI bf16x8 pack8s(const f32x16& a, float s) {
  bf16x8 r;
#pragma unroll
  for (int j = 0; j < 8; ++j) r[j] = (__bf16)(a[O + j] * s);
  return r;
}
DEVI f32x16 zero16() {
  f32x16 z;
#pragma unroll
  for (int i = 0; i < 16; ++i) z[i] = 0.f;
  return z;
}
DEVI bf16x8 load_wphi_frag(const u16* wphi, int head, int lane) {
  const int f = lane & 15, h = lane >> 5;
  const u16 sgn = (lane & 16) ? 0x8000 : 0;
  s16x8 r;
#pragma unroll
  for (int j = 0; j < 8; ++j) {
    const int d = 8 * (j >> 2) + 4 * h + (j & 3);
    r[j] = (short)(wphi[head * 256 + d * 16 + f] ^ sgn);
  }
  return __builtin_bit_cast(bf16x8, r);
}
DEVI bf16x8 wphi_frag_lds(const char* wl, int lane) {
  const char* p = wl + (4 * (lane >> 5) + ((lane & 15) >> 2)) * 32 + (lane & 3) * 8;
  const u32x4 r = __builtin_bit_cast(u32x4, tr_frag(p, p + 256));
  const unsigned sgn = (lane & 16) ? 0x80008000u : 0u;
  const u32x4 o = {r[0] ^ sgn, r[1] ^ sgn, r[2] ^ sgn, r[3] ^ sgn};
  return __builtin_bit_cast(bf16x8, o);
}
DEVI void dual_softmax(const f32x16& p, bf16x8& fp, bf16x8& fn) {
  float mp = fmaxf(fmaxf(fmaxf(p[0], p[1]), fmaxf(p[2], p[3])), fmaxf(fmaxf(p[4], p[5]), fmaxf(p[6], p[7])));
  float mn = fmaxf(fmaxf(fmaxf(p[8], p[9]), fmaxf(p[10], p[11])), fmaxf(fmaxf(p[12], p[13]), fmaxf(p[14], p[15])));
  mp = swap_max(mp) * L2E;
  mn = swap_max(mn) * L2E;
  f32x16 e;
  float sp = 0.f, sn = 0.f;
#pragma unroll
  for (int j = 0; j < 8; ++j) {
    e[j] = __builtin_amdgcn_exp2f(__builtin_fmaf(p[j], L2E, -mp));
    e[8 + j] = __builtin_amdgcn_exp2f(__builtin_fmaf(p[8 + j], L2E, -mn));
    sp += e[j];
    sn += e[8 + j];
  }
  sp = __builtin_amdgcn_rcpf(swap_add(sp));
  sn = __builtin_amdgcn_rcpf(swap_add(sn));
  fp = pack8s<0>(e, sp);
  fn = pack8s<8>(e, sn);
}

constexpr int STAGE_BYTES = 40960;
constexpr int K1_WPHI = 3 * STAGE_BYTES;
__global__ __launch_bounds__(512, 2) void k1_qkv(const u16* __restrict__ xA, const u16* __restrict__ wA,
                                                 const u16* __restrict__ wphi, const u16* __restrict__ xB,
                                                 const u16* __restrict__ wB, char* __restrict__ ws) {
  __shared__ __attribute__((aligned(16))) char smem[3 * STAGE_BYTES + 2048];
  const int tid = threadIdx.x, lane = tid & 63;
  const int w = __builtin_amdgcn_readfirstlane(tid >> 6);
  const int bid = blockIdx.x, xcd = bid & 7, idx = bid >> 3;
  const int br = xcd >> 2, jx = xcd & 3;
  const int ti = (jx & 1) * 8 + (idx & 7);
  const int g = (jx >> 1) * 4 + (idx >> 3);
  const u16* x = br ? xB : xA;
  const u16* W = br ? wB : wA;
  const int tt = w & 3, hp = w >> 2;
  const int h = lane >> 5;

  STAMP(0, 0);
  const uint32_t sm0 = lds_addr_of(smem);
  const uint32_t xo0 = (uint32_t)((ti * 128 + 16 * w + (lane >> 3)) * DM + (((lane & 7) ^ ((lane >> 4) & 7)) * 8)) * 2u;
  const uint32_t xo1 = (uint32_t)((ti * 128 + 16 * w + 8 + (lane >> 3)) * DM + (((lane & 7) ^ ((4 + (lane >> 4)) & 7)) * 8)) * 2u;
  const uint32_t wo0 = (uint32_t)((8 * w + (lane >> 3)) * 1536 + g * 64 + (((lane & 7) ^ (((lane >> 4) & 1) << 2)) * 8)) * 2u;
  auto stage_load = [&](int s, int buf) {
    const uint32_t b = sm0 + buf * STAGE_BYTES;
    glds16s(x + s * 64, xo0, b + w * 2048);
    glds16s(x + s * 64, xo1, b + w * 2048 + 1024);
    glds16s(W + (size_t)s * 64 * 1536, wo0, b + 16384 + w * 1024);
    glds16s(W + (size_t)s * 64 * 1536 + 512, wo0, b + 16384 + 8192 + w * 1024);
    glds16s(W + (size_t)s * 64 * 1536 + 1024, wo0, b + 16384 + 16384 + w * 1024);
  };
  if (br == 0 && w < 2) glds16s(wphi + (size_t)(4 * g) * 256 + w * 512, (uint32_t)lane * 16u, sm0 + K1_WPHI + w * 1024);
  stage_load(0, 0);
  stage_load(1, 1);

  const int xrow = 32 * tt + (lane & 31);
  const int xa0 = xrow * 128 + ((h ^ ((xrow >> 1) & 7)) * 16);
  const int blk = (lane >> 4) & 1, q4 = (lane & 15) >> 2, p4 = lane & 3;
  const int wa0 = 16384 + (8 * h + q4) * 128 + (((4 * hp + 2 * blk + (p4 >> 1)) ^ (((q4 >> 1) & 1) << 2)) * 16) + 8 * (p4 & 1);

  f32x16 aq = zero16(), ak = zero16(), av = zero16();
#pragma unroll
  for (int s = 0; s < 8; ++s) {
    if (s < 7) asm volatile("s_waitcnt vmcnt(5)" ::: "memory");
    else asm volatile("s_waitcnt vmcnt(0)" ::: "memory");
    __builtin_amdgcn_s_barrier();
    asm volatile("" ::: "memory");
    STAMP(0, 1 + s);
    if (s + 2 < 8) stage_load(s + 2, (s + 2) % 3);
    const char* b = smem + (s % 3) * STAGE_BYTES;
#pragma unroll
    for (int ks = 0; ks < 4; ++ks) {
      bf16x8 xf = lds_read16(b + (xa0 ^ (32 * ks)));
      const char* wb = b + wa0 + ks * 2048;
      bf16x8 fq = tr_frag(wb, wb + 512);
      bf16x8 fk = tr_frag(wb + 8192, wb + 8192 + 512);
      bf16x8 fv = tr_frag(wb + 16384, wb + 16384 + 512);
      aq = MFMA32(fq, xf, aq);
      ak = MFMA32(fk, xf, ak);
      av = MFMA32(xf, fv, av);
    }
    asm volatile("s_waitcnt lgkmcnt(0)" ::: "memory");
  }

  STAMP(0, 9);
  const int tile32 = ti * 4 + tt;
  const int head0 = 4 * g + 2 * hp;
  if (br == 1) {
    constexpr float QS = 0.25f * L2E;
    char* qb = ws + WS_QB + ((size_t)head0 * NT + tile32) * 1024 + lane * 16;
    *(bf16x8*)qb = pack8s<0>(aq, QS);
    *(bf16x8*)(qb + NT * 1024) = pack8s<8>(aq, QS);
    char* kb = ws + WS_KB + ((size_t)head0 * NT + tile32) * 1024 + lane * 16;
    *(bf16x8*)kb = pack8<0>(ak);
    *(bf16x8*)(kb + NT * 1024) = pack8<8>(ak);
    char* vb = ws + WS_VB + ((size_t)(head0 + ((lane >> 4) & 1)) * NT + tile32) * 1024 + h * 256 + (lane & 15) * 16;
    *(bf16x8*)vb = pack8<0>(av);
    *(bf16x8*)(vb + 512) = pack8<8>(av);
    STAMP(0, 10);
    return;
  }

  char* qa = ws + WS_QA + ((size_t)head0 * NT + tile32) * 1024 + lane * 16;
  *(bf16x8*)qa = pack8<0>(aq);
  *(bf16x8*)(qa + NT * 1024) = pack8<8>(aq);

  s16x8 i0, i1;
#pragma unroll
  for (int j = 0; j < 8; ++j) {
    const int f = 8 * (j >> 2) + 4 * h + (j & 3);
    i0[j] = ((lane & 31) == f) ? (short)0x3F80 : (short)0;
    i1[j] = ((lane & 31) == 16 + f) ? (short)0x3F80 : (short)0;
  }
  const bf16x8 id0 = __builtin_bit_cast(bf16x8, i0), id1 = __builtin_bit_cast(bf16x8, i1);
  const bf16x8 vf0 = pack8<0>(av), vf1 = pack8<8>(av);

  __builtin_amdgcn_s_barrier();
  float* red = (float*)smem;
  float* zred = (float*)(smem + 32768);
#pragma unroll
  for (int p = 0; p < 2; ++p) {
    const bf16x8 kf = p ? pack8<8>(ak) : pack8<0>(ak);
    f32x16 pk = MFMA32(wphi_frag_lds(smem + K1_WPHI + (2 * hp + p) * 512, lane), kf, zero16());
    bf16x8 fp, fn;
    dual_softmax(pk, fp, fn);
    f32x16 T = MFMA32(fp, id0, zero16());
    T = MFMA32(fn, id1, T);
    float z = 0.f;
#pragma unroll
    for (int r = 0; r < 16; ++r) z += T[r];
    z = swap_add(z);
    f32x16 kv = MFMA32(pack8<0>(T), vf0, zero16());
    kv = MFMA32(pack8<8>(T), vf1, kv);
    const int hl = 2 * hp + p;
    if (((lane >> 4) & 1) == p) {
      float* dst = red + (size_t)(tt * 4 + hl) * 512 + (h * 16 + (lane & 15)) * 4;
#pragma unroll
      for (int r4 = 0; r4 < 4; ++r4) {
        f32x4 v = {kv[4 * r4], kv[4 * r4 + 1], kv[4 * r4 + 2], kv[4 * r4 + 3]};
        *(f32x4*)(dst + r4 * 128) = v;
      }
    }
    if (lane < 32) zred[(tt * 4 + hl) * 32 + lane] = z;
  }
  __syncthreads();
  {
    const int hl = tid >> 7, pos = tid & 127;
    f32x4 s = *(const f32x4*)(red + (0 * 4 + hl) * 512 + pos * 4);
#pragma unroll
    for (int t = 1; t < 4; ++t) s += *(const f32x4*)(red + (t * 4 + hl) * 512 + pos * 4);
    float* dst = (float*)(ws + WS_KVP) + ((size_t)(4 * g + hl) * 16 + ti) * KVP_FLOATS;
    *(f32x4*)(dst + pos * 4) = s;
    if (tid < 128) {
      const int hz = tid >> 5, f = tid & 31;
      const float z = (zred[(0 * 4 + hz) * 32 + f] + zred[(1 * 4 + hz) * 32 + f]) +
                      (zred[(2 * 4 + hz) * 32 + f] + zred[(3 * 4 + hz) * 32 + f]);
      ((float*)(ws + WS_KVP))[((size_t)(4 * g + hz) * 16 + ti) * KVP_FLOATS + 512 + f] = z;
    }
  }
  STAMP(0, 10);
}

constexpr int K2_K = 0, K2_V = 16384, K2_ONES = 32768, K2_RED = 33792, K2_ZRED = K2_RED + 8192, K2_KVF = K2_ZRED + 256;
constexpr int K2_LDS = K2_KVF + 2048;
__global__ __launch_bounds__(512, 2) void k2_attn(const u16* __restrict__ wphi, char* __restrict__ ws) {
  __shared__ __attribute__((aligned(16))) char smem[K2_LDS];
  const int tid = threadIdx.x, lane = tid & 63;
  const int w = __builtin_amdgcn_readfirstlane(tid >> 6);
  const int head = blockIdx.x & 31, chunk = blockIdx.x >> 5;
  const int qt = chunk * 8 + w;
  const int h = lane >> 5;

  STAMP(1, 0);
  const bf16x8 qf = *(const bf16x8*)(ws + WS_QB + ((size_t)head * NT + qt) * 1024 + lane * 16);
  const bf16x8 qa = *(const bf16x8*)(ws + WS_QA + ((size_t)head * NT + qt) * 1024 + lane * 16);
  const bf16x8 wf = load_wphi_frag(wphi, head, lane);
  {
    const float* kvp = (const float*)(ws + WS_KVP) + (size_t)head * 16 * KVP_FLOATS;
    const int grp = tid >> 7, pos = tid & 127;
    f32x4 s0 = *(const f32x4*)(kvp + (grp * 4 + 0) * KVP_FLOATS + pos * 4);
    f32x4 s1 = *(const f32x4*)(kvp + (grp * 4 + 1) * KVP_FLOATS + pos * 4);
    f32x4 s2 = *(const f32x4*)(kvp + (grp * 4 + 2) * KVP_FLOATS + pos * 4);
    f32x4 s3 = *(const f32x4*)(kvp + (grp * 4 + 3) * KVP_FLOATS + pos * 4);
    f32x4 zz = {0.f, 0.f, 0.f, 0.f};
    if (tid < 128) zz = *(const f32x4*)(kvp + (tid >> 3) * KVP_FLOATS + 512 + (tid & 7) * 4);
    const int t0 = chunk * 8 - 8;
#pragma unroll
    for (int i = 0; i < 2; ++i) {
      const int lt = 2 * w + i;
      if (t0 + lt >= 0) {
        glds16(ws + WS_KB + ((size_t)head * NT + t0 + lt) * 1024 + lane * 16, smem + K2_K + lt * 1024);
        glds16(ws + WS_VB + ((size_t)head * NT + t0 + lt) * 1024 + lane * 16, smem + K2_V + lt * 1024);
      }
    }
    if (tid < 64) {
      u32x4 ones = {0x3F803F80u, 0x3F803F80u, 0x3F803F80u, 0x3F803F80u};
      *(u32x4*)(smem + K2_ONES + tid * 16) = ones;
    }
    *(f32x4*)(smem + K2_RED + grp * 2048 + pos * 16) = (s0 + s1) + (s2 + s3);
    if (tid < 128) {
#pragma unroll
      for (int c = 0; c < 4; ++c) {
        float v = zz[c];
        v += __shfl_xor(v, 8);
        v += __shfl_xor(v, 16);
        v += __shfl_xor(v, 32);
        zz[c] = v;
      }
      if (lane < 8) *(f32x4*)(smem + K2_ZRED + w * 128 + lane * 16) = zz;
    }
  }
  asm volatile("s_waitcnt vmcnt(0)" ::: "memory");
  __syncthreads();
  STAMP(1, 1);
  if (tid < 128) {
    const int pos = tid, r4 = pos >> 5, hd = pos & 31;
    f32x4 s = *(const f32x4*)(smem + K2_RED + pos * 16);
#pragma unroll
    for (int gq = 1; gq < 4; ++gq) s += *(const f32x4*)(smem + K2_RED + gq * 2048 + pos * 16);
    bf16x4 o;
#pragma unroll
    for (int c = 0; c < 4; ++c) o[c] = (__bf16)s[c];
    *(bf16x4*)(smem + K2_KVF + (r4 >> 1) * 1024 + ((hd >> 4) * 32 + (hd & 15)) * 16 + (r4 & 1) * 8) = o;
  }
  {
    const int f = tid & 31, dup = tid >> 5;
    const float* zr = (const float*)(smem + K2_ZRED);
    const float z = zr[f] + zr[32 + f];
    *(__bf16*)(smem + K2_KVF + (f >> 4) * 1024 + (((f >> 2) & 1) * 32 + 16 + dup) * 16 + (((f >> 3) & 1) * 4 + (f & 3)) * 2) = (__bf16)z;
  }

  {
    const char* kbase = smem + K2_K + w * 1024 + lane * 16;
    const bool lower = (lane & 16) == 0;
    const int vinc = lower ? 1024 : 0;
    const char* vbase = lower ? (smem + K2_V + w * 1024 + h * 256 + (lane & 15) * 16) : (smem + K2_ONES + (lane & 15) * 16);
    const int ql = lane & 31;
    const bool has0 = qt >= 8;
    f32x16 sm = MFMA32(lds_read16(kbase + 8 * 1024), qf, zero16());
    {
      f32x16 s0;
      if (has0) s0 = MFMA32(lds_read16(kbase), qf, zero16());
      else {
#pragma unroll
        for (int r = 0; r < 16; ++r) s0[r] = -INFINITY;
      }
#pragma unroll
      for (int r = 0; r < 16; ++r) {
        const int kl = (r & 3) + 8 * (r >> 2) + 4 * h;
        sm[r] = (kl <= ql) ? sm[r] : s0[r];
      }
    }
    float m = sm[0];
#pragma unroll
    for (int r = 1; r < 16; ++r) m = fmaxf(m, sm[r]);
#pragma unroll
    for (int i = 1; i < 8; ++i) {
      if (qt - 8 + i >= 0) {
        f32x16 s = MFMA32(lds_read16(kbase + i * 1024), qf, zero16());
#pragma unroll
        for (int r = 0; r < 16; r += 2) m = fmaxf(fmaxf(m, s[r]), s[r + 1]);
      }
    }
    m = swap_max(m);
    f32x16 negm;
#pragma unroll
    for (int r = 0; r < 16; ++r) negm[r] = -m;
    f32x16 o = zero16();
    {
      f32x16 e;
#pragma unroll
      for (int r = 0; r < 16; ++r) e[r] = __builtin_amdgcn_exp2f(sm[r] - m);
      const u32x4 pa = __builtin_bit_cast(u32x4, pack8<0>(e)), pb = __builtin_bit_cast(u32x4, pack8<8>(e));
      u32x4 da, db, ua, ub;
#pragma unroll
      for (int i = 0; i < 4; ++i) {
        const int ra = 2 * i, rb = 8 + 2 * i;
        const int ka = (ra & 3) + 8 * (ra >> 2) + 4 * h, kb = (rb & 3) + 8 * (rb >> 2) + 4 * h;
        const unsigned ma = (ka <= ql ? 0xFFFFu : 0u) | (ka + 1 <= ql ? 0xFFFF0000u : 0u);
        const unsigned mb = (kb <= ql ? 0xFFFFu : 0u) | (kb + 1 <= ql ? 0xFFFF0000u : 0u);
        da[i] = pa[i] & ma; ua[i] = pa[i] & ~ma;
        db[i] = pb[i] & mb; ub[i] = pb[i] & ~mb;
      }
      const char* vd = vbase + 8 * vinc;
      o = MFMA32(lds_read16(vd), __builtin_bit_cast(bf16x8, da), o);
      o = MFMA32(lds_read16(vd + 512), __builtin_bit_cast(bf16x8, db), o);
      if (has0) {
        o = MFMA32(lds_read16(vbase), __builtin_bit_cast(bf16x8, ua), o);
        o = MFMA32(lds_read16(vbase + 512), __builtin_bit_cast(bf16x8, ub), o);
      }
    }
#pragma unroll
    for (int i = 1; i < 8; ++i) {
      if (qt - 8 + i >= 0) {
        f32x16 s = MFMA32(lds_read16(kbase + i * 1024), qf, negm);
        f32x16 e;
#pragma unroll
        for (int r = 0; r < 16; ++r) e[r] = __builtin_amdgcn_exp2f(s[r]);
        const char* vp = vbase + i * vinc;
        o = MFMA32(lds_read16(vp), pack8<0>(e), o);
        o = MFMA32(lds_read16(vp + 512), pack8<8>(e), o);
      }
    }
    const float inv = __builtin_amdgcn_rcpf(o[8]);
    *(bf16x8*)(ws + WS_O + FRAG_BYTES + ((size_t)head * NT + qt) * 1024 + lane * 16) = pack8s<0>(o, inv);
  }

  STAMP(1, 2);
  __syncthreads();
  STAMP(1, 3);
  {
    f32x16 pq = MFMA32(wf, qa, zero16());
    bf16x8 fp, fn;
    dual_softmax(pq, fp, fn);
    f32x16 o = MFMA32(lds_read16(smem + K2_KVF + lane * 16), fp, zero16());
    o = MFMA32(lds_read16(smem + K2_KVF + 1024 + lane * 16), fn, o);
    const float inv = __builtin_amdgcn_rcpf(o[8] + 1e-6f);
    *(bf16x8*)(ws + WS_O + ((size_t)head * NT + qt) * 1024 + lane * 16) = pack8s<0>(o, inv);
  }
  STAMP(1, 4);
}

constexpr int K3_LDS = 65536;
__global__ __launch_bounds__(512, 2) void k3_out(const u16* __restrict__ woA, const u16* __restrict__ woB,
                                                 char* __restrict__ ws, u16* __restrict__ out) {
  __shared__ __attribute__((aligned(16))) char smem[K3_LDS];
  const int tid = threadIdx.x, lane = tid & 63;
  const int w = __builtin_amdgcn_readfirstlane(tid >> 6);
  const int xcd = blockIdx.x & 7, idx = blockIdx.x >> 3;
  const int br = xcd >> 2, ti = (xcd & 3) * 4 + (idx & 3), nj = idx >> 2;
  const u16* wo = br ? woB : woA;
  const int tt = w & 3, kh = w >> 2, h = lane >> 5;
  const int tile32 = ti * 4 + tt;

  STAMP(2, 0);
  const u16* wg = wo + (size_t)(256 * kh + 16 * tt + (lane >> 3)) * DM + nj * 64 + (((lane & 7) ^ (((lane >> 4) & 1) << 2)) * 8);
  const char* og = ws + WS_O + (size_t)br * FRAG_BYTES + ((size_t)(16 * kh) * NT + tile32) * 1024 + lane * 16;
  u32x4 wr[4][2];
  bf16x8 of[4][4];
#pragma unroll
  for (int c = 0; c < 4; ++c) {
    wr[c][0] = *(const u32x4*)(wg + (size_t)(64 * c) * DM);
    wr[c][1] = *(const u32x4*)(wg + (size_t)(64 * c + 8) * DM);
#pragma unroll
    for (int i = 0; i < 4; ++i) of[c][i] = *(const bf16x8*)(og + (size_t)(4 * c + i) * NT * 1024);
  }
  const int blk = (lane >> 4) & 1, q4 = (lane & 15) >> 2, p4 = lane & 3;
  f32x16 acc0 = zero16(), acc1 = zero16();
  char* wdst = smem + (256 * kh + 16 * tt) * 128 + lane * 16;
  const int ra0 = (256 * kh + 4 * h + q4) * 128 + (((2 * blk + (p4 >> 1)) ^ (((q4 >> 1) & 1) << 2)) * 16) + 8 * (p4 & 1);
  const int ra1 = (256 * kh + 4 * h + q4) * 128 + (((4 + 2 * blk + (p4 >> 1)) ^ (((q4 >> 1) & 1) << 2)) * 16) + 8 * (p4 & 1);
#pragma unroll
  for (int c = 0; c < 4; ++c) {
    *(u32x4*)(wdst + c * 64 * 128) = wr[c][0];
    *(u32x4*)(wdst + c * 64 * 128 + 1024) = wr[c][1];
    __syncthreads();
    STAMP(2, 1 + c);
#pragma unroll
    for (int i = 0; i < 4; ++i) {
      const int kb = (64 * c + 16 * i) * 128;
      const bf16x8 a0 = tr_frag(smem + ra0 + kb, smem + ra0 + kb + 1024);
      const bf16x8 a1 = tr_frag(smem + ra1 + kb, smem + ra1 + kb + 1024);
      acc0 = MFMA32(a0, of[c][i], acc0);
      acc1 = MFMA32(a1, of[c][i], acc1);
    }
  }
  __syncthreads();
  STAMP(2, 5);
  float* ex = (float*)smem + w * 1024 + lane * 4;
  const f32x16 give = kh ? acc0 : acc1;
  f32x16 keep = kh ? acc1 : acc0;
#pragma unroll
  for (int r4 = 0; r4 < 4; ++r4) {
    f32x4 v = {give[4 * r4], give[4 * r4 + 1], give[4 * r4 + 2], give[4 * r4 + 3]};
    *(f32x4*)(ex + r4 * 256) = v;
  }
  __syncthreads();
  const float* px = (const float*)smem + (w ^ 4) * 1024 + lane * 4;
#pragma unroll
  for (int r4 = 0; r4 < 4; ++r4) {
    const f32x4 v = *(const f32x4*)(px + r4 * 256);
#pragma unroll
    for (int c = 0; c < 4; ++c) keep[4 * r4 + c] += v[c];
  }
  const u32x4 lo = __builtin_bit_cast(u32x4, pack8<0>(keep)), hi = __builtin_bit_cast(u32x4, pack8<8>(keep));
  u16* op = out + (size_t)(tile32 * 32 + (lane & 31)) * 1024 + br * 512 + nj * 64 + kh * 32 + h * 8;
  {
    auto t0 = __builtin_amdgcn_permlane32_swap(lo[0], lo[2], false, false);
    auto t1 = __builtin_amdgcn_permlane32_swap(lo[1], lo[3], false, false);
    u32x4 v = {t0[0], t1[0], t0[1], t1[1]};
    *(u32x4*)op = v;
    auto t2 = __builtin_amdgcn_permlane32_swap(hi[0], hi[2], false, false);
    auto t3 = __builtin_amdgcn_permlane32_swap(hi[1], hi[3], false, false);
    u32x4 u = {t2[0], t3[0], t2[1], t3[1]};
    *(u32x4*)(op + 16) = u;
  }
  STAMP(2, 6);
}

extern "C" void kernel_launch(void* const* d_in, const int* in_sizes, int n_in, void* d_out, int out_size,
                              void* d_ws, size_t ws_size, hipStream_t stream) {
  const u16* xA = (const u16*)d_in[0];
  const u16* wA = (const u16*)d_in[1];
  const u16* wphi = (const u16*)d_in[2];
  const u16* woA = (const u16*)d_in[3];
  const u16* xB = (const u16*)d_in[4];
  const u16* wB = (const u16*)d_in[5];
  const u16* woB = (const u16*)d_in[6];
  char* ws = (char*)d_ws;
  k1_qkv<<<256, 512, 0, stream>>>(xA, wA, wphi, xB, wB, ws);
  k2_attn<<<256, 512, 0, stream>>>(wphi, ws);
  k3_out<<<256, 512, 0, stream>>>(woA, woB, ws, (u16*)d_out);
}
```

```cpp
#include <hip/hip_runtime.h>
#include <hip/hip_bf16.h>
#include <stdint.h>

typedef __bf16 bf16x8 __attribute__((ext_vector_type(8)));
typedef __bf16 bf16x4 __attribute__((ext_vector_type(4)));
typedef short s16x4 __attribute__((ext_vector_type(4)));
typedef short s16x8 __attribute__((ext_vector_type(8)));
typedef float f32x16 __attribute__((ext_vector_type(16)));
typedef float f32x4 __attribute__((ext_vector_type(4)));
typedef unsigned u32x4 __attribute__((ext_vector_type(4)));
typedef unsigned u32x2 __attribute__((ext_vector_type(2)));
typedef unsigned short u16;

#define LDS_AS __attribute__((address_space(3)))
#define GLB_AS __attribute__((address_space(1)))
#define DEVI __device__ __forceinline__
#define MFMA32(a, b, c) __builtin_amdgcn_mfma_f32_32x32x16_bf16((a), (b), (c), 0, 0, 0)

#define STAMP(i)
constexpr int SEQ = 2048, DM = 512, NH = 32, NT = 64;
constexpr float L2E = 1.4426950408889634f;

constexpr uint32_t FRAG_BYTES = NH * NT * 1024;
constexpr uint32_t WS_QKV = 0;
constexpr uint32_t WS_O = 6 * FRAG_BYTES;
constexpr uint32_t WS_KVP = 8 * FRAG_BYTES;
constexpr uint32_t KVP_BYTES = 544 * 4;
constexpr uint32_t WS_E = WS_KVP + NH * 8 * KVP_BYTES;
constexpr uint32_t WS_F = WS_E + 256 * 64 * 8;
constexpr uint32_t WS_END = WS_F + 256 * 64 * 8;

DEVI void glds16s(const void* sbase, uint32_t voff, uint32_t lds_addr) {
  asm volatile("s_mov_b32 m0, %2\n\ts_nop 0\n\tglobal_load_lds_dwordx4 %0, %1" ::"v"(voff), "s"(sbase), "s"(lds_addr) : "memory");
}
DEVI uint32_t lds_addr_of(const char* p) { return (uint32_t)(uintptr_t)(const LDS_AS char*)p; }
DEVI bf16x8 lds_read16(const char* p) { return *(const bf16x8*)p; }
DEVI bf16x8 tr_frag(const char* p0, const char* p1) {
  s16x4 a = __builtin_amdgcn_ds_read_tr16_b64_v4i16((LDS_AS s16x4*)p0);
  s16x4 b = __builtin_amdgcn_ds_read_tr16_b64_v4i16((LDS_AS s16x4*)p1);
  s16x8 r = __builtin_shufflevector(a, b, 0, 1, 2, 3, 4, 5, 6, 7);
  return __builtin_bit_cast(bf16x8, r);
}
DEVI float swap_max(float x) {
  auto t = __builtin_amdgcn_permlane32_swap(__float_as_uint(x), __float_as_uint(x), false, false);
  return fmaxf(__uint_as_float(t[0]), __uint_as_float(t[1]));
}
DEVI float swap_add(float x) {
  auto t = __builtin_amdgcn_permlane32_swap(__float_as_uint(x), __float_as_uint(x), false, false);
  return __uint_as_float(t[0]) + __uint_as_float(t[1]);
}
template <int O>
DEVI bf16x8 pack8(const f32x16& a) {
  bf16x8 r;
#pragma unroll
  for (int j = 0; j < 8; ++j) r[j] = (__bf16)a[O + j];
  return r;
}
template <int O>
DEVI bf16x8 pack8s(const f32x16& a, float s) {
  bf16x8 r;
#pragma unroll
  for (int j = 0; j < 8; ++j) r[j] = (__bf16)(a[O + j] * s);
  return r;
}
DEVI f32x16 zero16() {
  f32x16 z;
#pragma unroll
  for (int i = 0; i < 16; ++i) z[i] = 0.f;
  return z;
}
DEVI bf16x8 wphi_frag_lds(const char* wl, int lane) {
  const char* p = wl + (4 * (lane >> 5) + ((lane & 15) >> 2)) * 32 + (lane & 3) * 8;
  const u32x4 r = __builtin_bit_cast(u32x4, tr_frag(p, p + 256));
  const unsigned sgn = (lane & 16) ? 0x80008000u : 0u;
  const u32x4 o = {r[0] ^ sgn, r[1] ^ sgn, r[2] ^ sgn, r[3] ^ sgn};
  return __builtin_bit_cast(bf16x8, o);
}
DEVI void dual_softmax(const f32x16& p, bf16x8& fp, bf16x8& fn) {
  float mp = fmaxf(fmaxf(fmaxf(p[0], p[1]), fmaxf(p[2], p[3])), fmaxf(fmaxf(p[4], p[5]), fmaxf(p[6], p[7])));
  float mn = fmaxf(fmaxf(fmaxf(p[8], p[9]), fmaxf(p[10], p[11])), fmaxf(fmaxf(p[12], p[13]), fmaxf(p[14], p[15])));
  mp = swap_max(mp) * L2E;
  mn = swap_max(mn) * L2E;
  f32x16 e;
  float sp = 0.f, sn = 0.f;
#pragma unroll
  for (int j = 0; j < 8; ++j) {
    e[j] = __builtin_amdgcn_exp2f(__builtin_fmaf(p[j], L2E, -mp));
    e[8 + j] = __builtin_amdgcn_exp2f(__builtin_fmaf(p[8 + j], L2E, -mn));
    sp += e[j];
    sn += e[8 + j];
  }
  sp = __builtin_amdgcn_rcpf(swap_add(sp));
  sn = __builtin_amdgcn_rcpf(swap_add(sn));
  fp = pack8s<0>(e, sp);
  fn = pack8s<8>(e, sn);
}

typedef unsigned long long u64;
typedef GLB_AS u64 gu64;
#define RLX_AGENT __ATOMIC_RELAXED, __HIP_MEMORY_SCOPE_AGENT
constexpr int AUX_SC1 = 16;
DEVI u32x4 ld_sc1(__amdgpu_buffer_rsrc_t r, uint32_t off) { return __builtin_amdgcn_raw_buffer_load_b128(r, off, 0, AUX_SC1); }
DEVI bf16x8 ld_sc1_frag(__amdgpu_buffer_rsrc_t r, uint32_t off) { return __builtin_bit_cast(bf16x8, ld_sc1(r, off)); }
DEVI void st_sc1(__amdgpu_buffer_rsrc_t r, uint32_t off, u32x4 v) { __builtin_amdgcn_raw_buffer_store_b128(v, r, off, 0, AUX_SC1); }
DEVI void st_sc1_frag(__amdgpu_buffer_rsrc_t r, uint32_t off, bf16x8 v) { st_sc1(r, off, __builtin_bit_cast(u32x4, v)); }
DEVI void wg_barrier() {
  asm volatile("s_waitcnt lgkmcnt(0)" ::: "memory");
  __builtin_amdgcn_s_barrier();
  asm volatile("" ::: "memory");
}
DEVI void drain_barrier() {
  asm volatile("s_waitcnt vmcnt(0) lgkmcnt(0)" ::: "memory");
  __builtin_amdgcn_s_barrier();
  asm volatile("" ::: "memory");
}
DEVI u64 mix(u64 e) { return e * 0x9E3779B97F4A7C15ull + 0x632BE59BD9B4E019ull; }
DEVI void wait_slots(char* ws, uint32_t slot, u64 want, bool active) {
  const gu64* f = (const gu64*)(ws + WS_F) + slot;
  for (int it = 0; it < 200000; ++it) {
    const u64 v = active ? __hip_atomic_load(f, RLX_AGENT) : want;
    if (__builtin_amdgcn_ballot_w64(v != want) == 0) break;
    __builtin_amdgcn_s_sleep(1);
  }
  if (active) __hip_atomic_store((gu64*)(ws + WS_E) + slot, want, RLX_AGENT);
}
DEVI void signal_slots(char* ws, uint32_t slot, u64 val, bool active) {
  if (active) __hip_atomic_store((gu64*)(ws + WS_F) + slot, val, RLX_AGENT);
}

constexpr int STAGE_BYTES = 40960;
constexpr int L_WPHI = 3 * STAGE_BYTES;
constexpr int L_K = 0, L_V = 16384, L_ONES = 32768, L_RED = 33792, L_KVF = L_RED + 8 * 2176, L_WO = 57344;
constexpr int LDS_BYTES = L_WPHI + 1024;

__global__ __launch_bounds__(512, 2) void fused(const u16* __restrict__ xA, const u16* __restrict__ wA,
                                                const u16* __restrict__ wphi, const u16* __restrict__ woA,
                                                const u16* __restrict__ xB, const u16* __restrict__ wB,
                                                const u16* __restrict__ woB, char* __restrict__ ws,
                                                u16* __restrict__ out) {
  __shared__ __attribute__((aligned(16))) char smem[LDS_BYTES];
  const int tid = threadIdx.x, lane = tid & 63;
  const int w = __builtin_amdgcn_readfirstlane(tid >> 6);
  const int bid = blockIdx.x, xcd = bid & 7, idx = bid >> 3;
  const int h = lane >> 5;
  const int br1 = xcd >> 2, jx = xcd & 3;
  const int ti1 = (jx & 1) * 8 + (idx & 7);
  const int g1 = (jx >> 1) * 4 + (idx >> 3);
  const int head = bid & 31, chunk = bid >> 5;
  const int br3 = xcd >> 2, ti3 = (xcd & 3) * 4 + (idx & 3), nj = idx >> 2;
  const __amdgpu_buffer_rsrc_t wsr = __builtin_amdgcn_make_buffer_rsrc(ws, 0, WS_END, 0x00020000);
  const uint32_t sm0 = lds_addr_of(smem);

  STAMP(0);
  const uint32_t cslot = bid * 64 + lane;
  uint32_t pslot;
  bool pact;
  {
    const int cc = (ti1 >> 1) + (br1 ? (lane >> 2) : 0);
    const int s1 = br1 ? (ti1 - 2 * cc + 2) : (4 + (ti1 & 1));
    const int j = lane - 32, tj = 2 * chunk + ((j >> 3) & 1);
    const int c3 = ((j & 7) * 4 + (tj & 3)) * 8 + (j >> 4) * 4 + (tj >> 2);
    if (lane < 8) { pact = br1 ? (cc <= 7) : (lane < 4); pslot = (cc * 32 + 4 * g1 + (lane & 3)) * 64 + s1; }
    else if (lane < 16) { pact = true; pslot = ((lane - 8) * 32 + head) * 64 + 8 + chunk; }
    else if (lane < 32) { pact = false; pslot = 0; }
    else { pact = true; pslot = c3 * 64 + 32 + head; }
    if (!pact) pslot = cslot;
  }
  u64 cwant = 0, pval = 0;
  if (w == 0) {
    cwant = __hip_atomic_load((const gu64*)(ws + WS_E) + cslot, RLX_AGENT);
    pval = __hip_atomic_load((const gu64*)(ws + WS_E) + pslot, RLX_AGENT);
  }

  {
    const u16* x = br1 ? xB : xA;
    const u16* W = br1 ? wB : wA;
    const int tt = w & 3, hp = w >> 2;
    const uint32_t xo0 = (uint32_t)((ti1 * 128 + 16 * w + (lane >> 3)) * DM + (((lane & 7) ^ ((lane >> 4) & 7)) * 8)) * 2u;
    const uint32_t xo1 = (uint32_t)((ti1 * 128 + 16 * w + 8 + (lane >> 3)) * DM + (((lane & 7) ^ ((4 + (lane >> 4)) & 7)) * 8)) * 2u;
    const uint32_t wo0 = (uint32_t)((8 * w + (lane >> 3)) * 1536 + g1 * 64 + (((lane & 7) ^ (((lane >> 4) & 1) << 2)) * 8)) * 2u;
    auto stage_load = [&](int s, int buf) {
      const uint32_t b = sm0 + buf * STAGE_BYTES;
      glds16s(x + s * 64, xo0, b + w * 2048);
      glds16s(x + s * 64, xo1, b + w * 2048 + 1024);
      glds16s(W + (size_t)s * 64 * 1536, wo0, b + 16384 + w * 1024);
      glds16s(W + (size_t)s * 64 * 1536 + 512, wo0, b + 16384 + 8192 + w * 1024);
      glds16s(W + (size_t)s * 64 * 1536 + 1024, wo0, b + 16384 + 16384 + w * 1024);
    };
    if (w == 1) glds16s(wphi + (size_t)(head & ~1) * 256, (uint32_t)lane * 16u, sm0 + L_WPHI);
    stage_load(0, 0);
    stage_load(1, 1);

    const int xrow = 32 * tt + (lane & 31);
    const int xa0 = xrow * 128 + ((h ^ ((xrow >> 1) & 7)) * 16);
    const int blk = (lane >> 4) & 1, q4 = (lane & 15) >> 2, p4 = lane & 3;
    const int wa0 = 16384 + (8 * h + q4) * 128 + (((4 * hp + 2 * blk + (p4 >> 1)) ^ (((q4 >> 1) & 1) << 2)) * 16) + 8 * (p4 & 1);

    f32x16 aq = zero16(), ak = zero16(), av = zero16();
#pragma unroll
    for (int s = 0; s < 8; ++s) {
      if (s < 7) asm volatile("s_waitcnt vmcnt(5)" ::: "memory");
      else asm volatile("s_waitcnt vmcnt(0)" ::: "memory");
      __builtin_amdgcn_s_barrier();
      asm volatile("" ::: "memory");
      if (s == 0) STAMP(1);
      if (s + 2 < 8) stage_load(s + 2, (s + 2) % 3);
      const char* b = smem + (s % 3) * STAGE_BYTES;
#pragma unroll
      for (int ks = 0; ks < 4; ++ks) {
        bf16x8 xf = lds_read16(b + (xa0 ^ (32 * ks)));
        const char* wb = b + wa0 + ks * 2048;
        bf16x8 fq = tr_frag(wb, wb + 512);
        bf16x8 fk = tr_frag(wb + 8192, wb + 8192 + 512);
        bf16x8 fv = tr_frag(wb + 16384, wb + 16384 + 512);
        aq = MFMA32(fq, xf, aq);
        ak = MFMA32(fk, xf, ak);
        av = MFMA32(xf, fv, av);
      }
      asm volatile("s_waitcnt lgkmcnt(0)" ::: "memory");
    }
    STAMP(2);
    const int tile32 = ti1 * 4 + tt, head0 = 4 * g1 + 2 * hp;
    const float qs = br1 ? 0.25f * L2E : 1.0f;
    const uint32_t base = WS_QKV + (br1 ? 3 : 0) * FRAG_BYTES;
    const uint32_t qo = base + (head0 * NT + tile32) * 1024 + lane * 16;
    st_sc1_frag(wsr, qo, pack8s<0>(aq, qs));
    st_sc1_frag(wsr, qo + NT * 1024, pack8s<8>(aq, qs));
    st_sc1_frag(wsr, qo + FRAG_BYTES, pack8<0>(ak));
    st_sc1_frag(wsr, qo + FRAG_BYTES + NT * 1024, pack8<8>(ak));
    const uint32_t vo = base + 2 * FRAG_BYTES + ((head0 + ((lane >> 4) & 1)) * NT + tile32) * 1024 + h * 256 + (lane & 15) * 16;
    st_sc1_frag(wsr, vo, pack8<0>(av));
    st_sc1_frag(wsr, vo + 512, pack8<8>(av));
    drain_barrier();
    STAMP(3);
    if (w == 0) {
      cwant = mix(cwant);
      pval = mix(pval);
      signal_slots(ws, pslot, pval, pact && lane < 8);
      wait_slots(ws, cslot, cwant, lane < 4 ? (2 * chunk - 2 + lane >= 0) : lane < 6);
    }
    if (tid >= 448) {
      const u32x4 ones = {0x3F803F80u, 0x3F803F80u, 0x3F803F80u, 0x3F803F80u};
      *(u32x4*)(smem + L_ONES + (tid - 448) * 16) = ones;
    }
    wg_barrier();
    STAMP(4);
  }

  const int qt = chunk * 8 + w;
  const bool lower = (lane & 16) == 0;
  bf16x8 qf, qa;
  {
    const uint32_t to = (head * NT + qt) * 1024;
    const bf16x8 ka = ld_sc1_frag(wsr, WS_QKV + 1 * FRAG_BYTES + to + lane * 16);
    u32x4 va0 = ld_sc1(wsr, WS_QKV + 2 * FRAG_BYTES + to + h * 256 + (lane & 15) * 16);
    u32x4 va1 = ld_sc1(wsr, WS_QKV + 2 * FRAG_BYTES + to + 512 + h * 256 + (lane & 15) * 16);
    const int t0 = chunk * 8 - 8;
    u32x4 kv[4];
    if (t0 + 2 * w >= 0) {
      const uint32_t o = (head * NT + t0 + 2 * w) * 1024 + lane * 16;
      kv[0] = ld_sc1(wsr, WS_QKV + 4 * FRAG_BYTES + o);
      kv[1] = ld_sc1(wsr, WS_QKV + 4 * FRAG_BYTES + o + 1024);
      kv[2] = ld_sc1(wsr, WS_QKV + 5 * FRAG_BYTES + o);
      kv[3] = ld_sc1(wsr, WS_QKV + 5 * FRAG_BYTES + o + 1024);
    }
    qf = ld_sc1_frag(wsr, WS_QKV + 3 * FRAG_BYTES + to + lane * 16);
    qa = ld_sc1_frag(wsr, WS_QKV + 0 * FRAG_BYTES + to + lane * 16);
    const bf16x8 wf0 = wphi_frag_lds(smem + L_WPHI + (head & 1) * 512, lane);

    s16x8 i0, i1;
#pragma unroll
    for (int j = 0; j < 8; ++j) {
      const int f = 8 * (j >> 2) + 4 * h + (j & 3);
      i0[j] = ((lane & 31) == f) ? (short)0x3F80 : (short)0;
      i1[j] = ((lane & 31) == 16 + f) ? (short)0x3F80 : (short)0;
    }
    const bf16x8 id0 = __builtin_bit_cast(bf16x8, i0), id1 = __builtin_bit_cast(bf16x8, i1);
    const u32x4 ones = {0x3F803F80u, 0x3F803F80u, 0x3F803F80u, 0x3F803F80u};
    if (!lower) { va0 = ones; va1 = ones; }
    f32x16 pk = MFMA32(wf0, ka, zero16());
    bf16x8 fp, fn;
    dual_softmax(pk, fp, fn);
    f32x16 T = MFMA32(fp, id0, zero16());
    T = MFMA32(fn, id1, T);
    f32x16 kvp = MFMA32(pack8<0>(T), __builtin_bit_cast(bf16x8, va0), zero16());
    kvp = MFMA32(pack8<8>(T), __builtin_bit_cast(bf16x8, va1), kvp);
    if ((lane & 31) <= 16) {
      char* dst = smem + L_RED + w * 2176 + (lower ? (h * 16 + (lane & 15)) * 16 : 2048 + h * 16);
      const int st = lower ? 512 : 32;
#pragma unroll
      for (int r4 = 0; r4 < 4; ++r4) {
        f32x4 v = {kvp[4 * r4], kvp[4 * r4 + 1], kvp[4 * r4 + 2], kvp[4 * r4 + 3]};
        *(f32x4*)(dst + r4 * st) = v;
      }
    }
    if (t0 + 2 * w >= 0) {
      *(u32x4*)(smem + L_K + 2 * w * 1024 + lane * 16) = kv[0];
      *(u32x4*)(smem + L_K + 2 * w * 1024 + 1024 + lane * 16) = kv[1];
      *(u32x4*)(smem + L_V + 2 * w * 1024 + lane * 16) = kv[2];
      *(u32x4*)(smem + L_V + 2 * w * 1024 + 1024 + lane * 16) = kv[3];
    }
    wg_barrier();
    STAMP(5);
    if (w == 0) {
#pragma unroll
      for (int p = 0; p < 3; ++p) {
        if (p < 2 || lane < 8) {
          const char* src = smem + L_RED + p * 1024 + lane * 16;
          f32x4 s = *(const f32x4*)src;
#pragma unroll
          for (int t = 1; t < 8; ++t) s += *(const f32x4*)(src + t * 2176);
          st_sc1(wsr, WS_KVP + (head * 8 + chunk) * KVP_BYTES + p * 1024 + lane * 16, __builtin_bit_cast(u32x4, s));
        }
      }
    }
  }
  const u16* wo = br3 ? woB : woA;
  auto wo_load = [&]() {
    const uint32_t off = (uint32_t)((64 * w + (lane >> 3)) * DM + nj * 64 + (((lane & 7) ^ (((lane >> 4) & 1) << 2)) * 8)) * 2u;
#pragma unroll
    for (int i = 0; i < 8; ++i) glds16s(wo + i * 8 * DM, off, sm0 + L_WO + w * 8192 + i * 1024);
  };
  if (w != 0) wo_load();

  {
    const char* kbase = smem + L_K + w * 1024 + lane * 16;
    const int vinc = lower ? 1024 : 0;
    const char* vbase = lower ? (smem + L_V + w * 1024 + h * 256 + (lane & 15) * 16) : (smem + L_ONES + (lane & 15) * 16);
    const int ql = lane & 31;
    const bool has0 = qt >= 8;
    f32x16 sm = MFMA32(lds_read16(kbase + 8 * 1024), qf, zero16());
    {
      f32x16 s0;
      if (has0) s0 = MFMA32(lds_read16(kbase), qf, zero16());
      else {
#pragma unroll
        for (int r = 0; r < 16; ++r) s0[r] = -INFINITY;
      }
#pragma unroll
      for (int r = 0; r < 16; ++r) {
        const int kl = (r & 3) + 8 * (r >> 2) + 4 * h;
        sm[r] = (kl <= ql) ? sm[r] : s0[r];
      }
    }
    float m = sm[0];
#pragma unroll
    for (int r = 1; r < 16; ++r) m = fmaxf(m, sm[r]);
#pragma unroll
    for (int i = 1; i < 8; ++i) {
      if (qt - 8 + i >= 0) {
        f32x16 s = MFMA32(lds_read16(kbase + i * 1024), qf, zero16());
#pragma unroll
        for (int r = 0; r < 16; r += 2) m = fmaxf(fmaxf(m, s[r]), s[r + 1]);
      }
    }
    m = swap_max(m);
    if (w == 0) {
      asm volatile("s_waitcnt vmcnt(0)" ::: "memory");
      signal_slots(ws, pslot, pval, lane >= 8 && lane < 16);
      wo_load();
    }
    f32x16 negm;
#pragma unroll
    for (int r = 0; r < 16; ++r) negm[r] = -m;
    f32x16 o = zero16();
    {
      f32x16 e;
#pragma unroll
      for (int r = 0; r < 16; ++r) e[r] = __builtin_amdgcn_exp2f(sm[r] - m);
      const u32x4 pa = __builtin_bit_cast(u32x4, pack8<0>(e)), pb = __builtin_bit_cast(u32x4, pack8<8>(e));
      u32x4 da, db, ua, ub;
#pragma unroll
      for (int i = 0; i < 4; ++i) {
        const int ra = 2 * i, rb = 8 + 2 * i;
        const int ka = (ra & 3) + 8 * (ra >> 2) + 4 * h, kb = (rb & 3) + 8 * (rb >> 2) + 4 * h;
        const unsigned ma = (ka <= ql ? 0xFFFFu : 0u) | (ka + 1 <= ql ? 0xFFFF0000u : 0u);
        const unsigned mb = (kb <= ql ? 0xFFFFu : 0u) | (kb + 1 <= ql ? 0xFFFF0000u : 0u);
        da[i] = pa[i] & ma; ua[i] = pa[i] & ~ma;
        db[i] = pb[i] & mb; ub[i] = pb[i] & ~mb;
      }
      const char* vd = vbase + 8 * vinc;
      o = MFMA32(lds_read16(vd), __builtin_bit_cast(bf16x8, da), o);
      o = MFMA32(lds_read16(vd + 512), __builtin_bit_cast(bf16x8, db), o);
      if (has0) {
        o = MFMA32(lds_read16(vbase), __builtin_bit_cast(bf16x8, ua), o);
        o = MFMA32(lds_read16(vbase + 512), __builtin_bit_cast(bf16x8, ub), o);
      }
    }
#pragma unroll
    for (int i = 1; i < 8; ++i) {
      if (qt - 8 + i >= 0) {
        f32x16 s = MFMA32(lds_read16(kbase + i * 1024), qf, negm);
        f32x16 e;
#pragma unroll
        for (int r = 0; r < 16; ++r) e[r] = __builtin_amdgcn_exp2f(s[r]);
        const char* vp = vbase + i * vinc;
        o = MFMA32(lds_read16(vp), pack8<0>(e), o);
        o = MFMA32(lds_read16(vp + 512), pack8<8>(e), o);
      }
    }
    const float inv = __builtin_amdgcn_rcpf(o[8]);
    st_sc1_frag(wsr, WS_O + FRAG_BYTES + (head * NT + qt) * 1024 + lane * 16, pack8s<0>(o, inv));
  }
  STAMP(6);

  {
    if (w == 0) wait_slots(ws, cslot, cwant, lane >= 8 && lane < 16);
    const bf16x8 wf = wphi_frag_lds(smem + L_WPHI + (head & 1) * 512, lane);
    f32x16 pq = MFMA32(wf, qa, zero16());
    bf16x8 fp, fn;
    dual_softmax(pq, fp, fn);
    wg_barrier();
    STAMP(7);
    const int grp = tid >> 7, pos = tid & 127;
    const uint32_t kb = WS_KVP + head * 8 * KVP_BYTES;
    const f32x4 s0 = __builtin_bit_cast(f32x4, ld_sc1(wsr, kb + (2 * grp) * KVP_BYTES + pos * 16));
    const f32x4 s1 = __builtin_bit_cast(f32x4, ld_sc1(wsr, kb + (2 * grp + 1) * KVP_BYTES + pos * 16));
    if (w == 0) {
      f32x4 zz = __builtin_bit_cast(f32x4, ld_sc1(wsr, kb + (lane >> 3) * KVP_BYTES + 2048 + (lane & 7) * 16));
#pragma unroll
      for (int c = 0; c < 4; ++c) {
        float v = zz[c];
        v += __shfl_xor(v, 8);
        v += __shfl_xor(v, 16);
        v += __shfl_xor(v, 32);
        zz[c] = v;
      }
      if (lane < 8) *(f32x4*)(smem + L_RED + 8192 + lane * 16) = zz;
    }
    *(f32x4*)(smem + L_RED + grp * 2048 + pos * 16) = s0 + s1;
    wg_barrier();
    if (tid < 128) {
      const int r4 = pos >> 5, hd = pos & 31;
      f32x4 s = *(const f32x4*)(smem + L_RED + pos * 16);
#pragma unroll
      for (int gq = 1; gq < 4; ++gq) s += *(const f32x4*)(smem + L_RED + gq * 2048 + pos * 16);
      bf16x4 o;
#pragma unroll
      for (int c = 0; c < 4; ++c) o[c] = (__bf16)s[c];
      *(bf16x4*)(smem + L_KVF + (r4 >> 1) * 1024 + ((hd >> 4) * 32 + (hd & 15)) * 16 + (r4 & 1) * 8) = o;
    }
    {
      const int f = tid & 31, dup = tid >> 5;
      const float z = ((const float*)(smem + L_RED + 8192))[f];
      *(__bf16*)(smem + L_KVF + (f >> 4) * 1024 + (((f >> 2) & 1) * 32 + 16 + dup) * 16 + (((f >> 3) & 1) * 4 + (f & 3)) * 2) = (__bf16)z;
    }
    wg_barrier();
    f32x16 o = MFMA32(lds_read16(smem + L_KVF + lane * 16), fp, zero16());
    o = MFMA32(lds_read16(smem + L_KVF + 1024 + lane * 16), fn, o);
    const float inv = __builtin_amdgcn_rcpf(o[8] + 1e-6f);
    st_sc1_frag(wsr, WS_O + (head * NT + qt) * 1024 + lane * 16, pack8s<0>(o, inv));
    STAMP(8);
    drain_barrier();
    STAMP(9);
    if (w == 0) {
      signal_slots(ws, pslot, pval, lane >= 32);
      wait_slots(ws, cslot, cwant, lane >= 32);
    }
    wg_barrier();
    STAMP(10);
  }

  {
    const int tt = w & 3, kh = w >> 2;
    const int tile32 = ti3 * 4 + tt;
    const uint32_t og = WS_O + br3 * FRAG_BYTES + ((16 * kh) * NT + tile32) * 1024 + lane * 16;
    bf16x8 of[16];
#pragma unroll
    for (int i = 0; i < 16; ++i) of[i] = ld_sc1_frag(wsr, og + i * NT * 1024);
    const int blk = (lane >> 4) & 1, q4 = (lane & 15) >> 2, p4 = lane & 3;
    f32x16 acc0 = zero16(), acc1 = zero16();
    const char* wl = smem + L_WO;
    const int ra0 = (256 * kh + 4 * h + q4) * 128 + (((2 * blk + (p4 >> 1)) ^ (((q4 >> 1) & 1) << 2)) * 16) + 8 * (p4 & 1);
    const int ra1 = (256 * kh + 4 * h + q4) * 128 + (((4 + 2 * blk + (p4 >> 1)) ^ (((q4 >> 1) & 1) << 2)) * 16) + 8 * (p4 & 1);
#pragma unroll
    for (int i = 0; i < 16; ++i) {
      const int kb = 16 * i * 128;
      const bf16x8 a0 = tr_frag(wl + ra0 + kb, wl + ra0 + kb + 1024);
      const bf16x8 a1 = tr_frag(wl + ra1 + kb, wl + ra1 + kb + 1024);
      acc0 = MFMA32(a0, of[i], acc0);
      acc1 = MFMA32(a1, of[i], acc1);
    }
    STAMP(11);
    float* ex = (float*)smem + w * 1024 + lane * 4;
    const f32x16 give = kh ? acc0 : acc1;
    f32x16 keep = kh ? acc1 : acc0;
#pragma unroll
    for (int r4 = 0; r4 < 4; ++r4) {
      f32x4 v = {give[4 * r4], give[4 * r4 + 1], give[4 * r4 + 2], give[4 * r4 + 3]};
      *(f32x4*)(ex + r4 * 256) = v;
    }
    wg_barrier();
    const float* px = (const float*)smem + (w ^ 4) * 1024 + lane * 4;
#pragma unroll
    for (int r4 = 0; r4 < 4; ++r4) {
      const f32x4 v = *(const f32x4*)(px + r4 * 256);
#pragma unroll
      for (int c = 0; c < 4; ++c) keep[4 * r4 + c] += v[c];
    }
    const u32x4 lo = __builtin_bit_cast(u32x4, pack8<0>(keep)), hi = __builtin_bit_cast(u32x4, pack8<8>(keep));
    u16* op = out + (size_t)(tile32 * 32 + (lane & 31)) * 1024 + br3 * 512 + nj * 64 + kh * 32 + h * 8;
    auto t0 = __builtin_amdgcn_permlane32_swap(lo[0], lo[2], false, false);
    auto t1 = __builtin_amdgcn_permlane32_swap(lo[1], lo[3], false, false);
    u32x4 v = {t0[0], t1[0], t0[1], t1[1]};
    *(u32x4*)op = v;
    auto t2 = __builtin_amdgcn_permlane32_swap(hi[0], hi[2], false, false);
    auto t3 = __builtin_amdgcn_permlane32_swap(hi[1], hi[3], false, false);
    u32x4 u = {t2[0], t3[0], t2[1], t3[1]};
    *(u32x4*)(op + 16) = u;
  }
  STAMP(12);
}

extern "C" void kernel_launch(void* const* d_in, const int* in_sizes, int n_in, void* d_out, int out_size,
                              void* d_ws, size_t ws_size, hipStream_t stream) {
  fused<<<256, 512, 0, stream>>>((const u16*)d_in[0], (const u16*)d_in[1], (const u16*)d_in[2], (const u16*)d_in[3],
                                 (const u16*)d_in[4], (const u16*)d_in[5], (const u16*)d_in[6], (char*)d_ws, (u16*)d_out);
}
```

```cpp
#include <hip/hip_runtime.h>
#include <hip/hip_bf16.h>
#include <stdint.h>

typedef __bf16 bf16x8 __attribute__((ext_vector_type(8)));
typedef __bf16 bf16x4 __attribute__((ext_vector_type(4)));
typedef short s16x4 __attribute__((ext_vector_type(4)));
typedef short s16x8 __attribute__((ext_vector_type(8)));
typedef float f32x16 __attribute__((ext_vector_type(16)));
typedef float f32x4 __attribute__((ext_vector_type(4)));
typedef unsigned u32x4 __attribute__((ext_vector_type(4)));
typedef unsigned u32x2 __attribute__((ext_vector_type(2)));
typedef unsigned short u16;

#define LDS_AS __attribute__((address_space(3)))
#define GLB_AS __attribute__((address_space(1)))
#define DEVI __device__ __forceinline__
#define MFMA32(a, b, c) __builtin_amdgcn_mfma_f32_32x32x16_bf16((a), (b), (c), 0, 0, 0)

#define STAMP(i)
constexpr int SEQ = 2048, DM = 512, NH = 32, NT = 64;
constexpr float L2E = 1.4426950408889634f;

constexpr uint32_t FRAG_BYTES = NH * NT * 1024;
constexpr uint32_t WS_K = 0, WS_V = FRAG_BYTES;
constexpr uint32_t WS_O = 2 * FRAG_BYTES;
constexpr uint32_t WS_KVP = 4 * FRAG_BYTES;
constexpr uint32_t KVP_BYTES = 1088;
constexpr uint32_t WS_E = WS_KVP + NH * 16 * KVP_BYTES;
constexpr uint32_t WS_F = WS_E + 256 * 64 * 8;
constexpr uint32_t WS_END = WS_F + 256 * 64 * 8;

DEVI void glds16s(const void* sbase, uint32_t voff, uint32_t lds_addr) {
  asm volatile("s_mov_b32 m0, %2\n\ts_nop 0\n\tglobal_load_lds_dwordx4 %0, %1" ::"v"(voff), "s"(sbase), "s"(lds_addr) : "memory");
}
DEVI uint32_t lds_addr_of(const char* p) { return (uint32_t)(uintptr_t)(const LDS_AS char*)p; }
DEVI bf16x8 lds_read16(const char* p) { return *(const bf16x8*)p; }
DEVI bf16x8 tr_frag(const char* p0, const char* p1) {
  s16x4 a = __builtin_amdgcn_ds_read_tr16_b64_v4i16((LDS_AS s16x4*)p0);
  s16x4 b = __builtin_amdgcn_ds_read_tr16_b64_v4i16((LDS_AS s16x4*)p1);
  s16x8 r = __builtin_shufflevector(a, b, 0, 1, 2, 3, 4, 5, 6, 7);
  return __builtin_bit_cast(bf16x8, r);
}
DEVI float swap_max(float x) {
  auto t = __builtin_amdgcn_permlane32_swap(__float_as_uint(x), __float_as_uint(x), false, false);
  return fmaxf(__uint_as_float(t[0]), __uint_as_float(t[1]));
}
DEVI float swap_add(float x) {
  auto t = __builtin_amdgcn_permlane32_swap(__float_as_uint(x), __float_as_uint(x), false, false);
  return __uint_as_float(t[0]) + __uint_as_float(t[1]);
}
template <int O>
DEVI bf16x8 pack8(const f32x16& a) {
  bf16x8 r;
#pragma unroll
  for (int j = 0; j < 8; ++j) r[j] = (__bf16)a[O + j];
  return r;
}
template <int O>
DEVI bf16x8 pack8s(const f32x16& a, float s) {
  bf16x8 r;
#pragma unroll
  for (int j = 0; j < 8; ++j) r[j] = (__bf16)(a[O + j] * s);
  return r;
}
DEVI f32x16 zero16() {
  f32x16 z;
#pragma unroll
  for (int i = 0; i < 16; ++i) z[i] = 0.f;
  return z;
}
DEVI bf16x8 wphi_frag_lds(const char* wl, int lane) {
  const char* p = wl + (4 * (lane >> 5) + ((lane & 15) >> 2)) * 32 + (lane & 3) * 8;
  const u32x4 r = __builtin_bit_cast(u32x4, tr_frag(p, p + 256));
  const unsigned sgn = (lane & 16) ? 0x80008000u : 0u;
  const u32x4 o = {r[0] ^ sgn, r[1] ^ sgn, r[2] ^ sgn, r[3] ^ sgn};
  return __builtin_bit_cast(bf16x8, o);
}
DEVI void dual_softmax(const f32x16& p, bf16x8& fp, bf16x8& fn) {
  float mp = fmaxf(fmaxf(fmaxf(p[0], p[1]), fmaxf(p[2], p[3])), fmaxf(fmaxf(p[4], p[5]), fmaxf(p[6], p[7])));
  float mn = fmaxf(fmaxf(fmaxf(p[8], p[9]), fmaxf(p[10], p[11])), fmaxf(fmaxf(p[12], p[13]), fmaxf(p[14], p[15])));
  mp = swap_max(mp) * L2E;
  mn = swap_max(mn) * L2E;
  f32x16 e;
  float sp = 0.f, sn = 0.f;
#pragma unroll
  for (int j = 0; j < 8; ++j) {
    e[j] = __builtin_amdgcn_exp2f(__builtin_fmaf(p[j], L2E, -mp));
    e[8 + j] = __builtin_amdgcn_exp2f(__builtin_fmaf(p[8 + j], L2E, -mn));
    sp += e[j];
    sn += e[8 + j];
  }
  sp = __builtin_amdgcn_rcpf(swap_add(sp));
  sn = __builtin_amdgcn_rcpf(swap_add(sn));
  fp = pack8s<0>(e, sp);
  fn = pack8s<8>(e, sn);
}

typedef unsigned long long u64;
typedef GLB_AS u64 gu64;
#define RLX_AGENT __ATOMIC_RELAXED, __HIP_MEMORY_SCOPE_AGENT
constexpr int AUX_SC1 = 16;
DEVI u32x4 ld_sc1(__amdgpu_buffer_rsrc_t r, uint32_t off) { return __builtin_amdgcn_raw_buffer_load_b128(r, off, 0, AUX_SC1); }
DEVI bf16x8 ld_sc1_frag(__amdgpu_buffer_rsrc_t r, uint32_t off) { return __builtin_bit_cast(bf16x8, ld_sc1(r, off)); }
DEVI void st_sc1(__amdgpu_buffer_rsrc_t r, uint32_t off, u32x4 v) { __builtin_amdgcn_raw_buffer_store_b128(v, r, off, 0, AUX_SC1); }
DEVI void st_sc1_frag(__amdgpu_buffer_rsrc_t r, uint32_t off, bf16x8 v) { st_sc1(r, off, __builtin_bit_cast(u32x4, v)); }
DEVI void wg_barrier() {
  asm volatile("s_waitcnt lgkmcnt(0)" ::: "memory");
  __builtin_amdgcn_s_barrier();
  asm volatile("" ::: "memory");
}
DEVI void drain_barrier() {
  asm volatile("s_waitcnt vmcnt(0) lgkmcnt(0)" ::: "memory");
  __builtin_amdgcn_s_barrier();
  asm volatile("" ::: "memory");
}
DEVI u64 mix(u64 e) { return e * 0x9E3779B97F4A7C15ull + 0x632BE59BD9B4E019ull; }
DEVI void wait_slots(char* ws, uint32_t slot, u64 want, bool active) {
  const gu64* f = (const gu64*)(ws + WS_F) + slot;
  for (int it = 0; it < 200000; ++it) {
    const u64 v = active ? __hip_atomic_load(f, RLX_AGENT) : want;
    if (__builtin_amdgcn_ballot_w64(v != want) == 0) break;
    __builtin_amdgcn_s_sleep(1);
  }
  if (active) __hip_atomic_store((gu64*)(ws + WS_E) + slot, want, RLX_AGENT);
}
DEVI void signal_slots(char* ws, uint32_t slot, u64 val, bool active) {
  if (active) __hip_atomic_store((gu64*)(ws + WS_F) + slot, val, RLX_AGENT);
}

constexpr int STAGE_BYTES = 28672;
constexpr int L_EX = 4 * STAGE_BYTES, EX_SLOT = 6144;
constexpr int L_WO = 0, L_K = 65536, L_V = L_K + 24576, L_ONES = L_EX + 4096;
constexpr int L_RED2 = L_K, L_ZRED = L_RED2 + 16384, L_KVF = L_ZRED + 1024;
constexpr int LDS_BYTES = L_EX + 8 * EX_SLOT;

DEVI int bid_of(int hp, int ti) { return ((hp & 7) * 4 + (ti & 3)) * 8 + (ti >> 2) * 2 + (hp >> 3); }

__global__ __launch_bounds__(512, 2) void fused(const u16* __restrict__ xA, const u16* __restrict__ wA,
                                                const u16* __restrict__ wphi, const u16* __restrict__ woA,
                                                const u16* __restrict__ xB, const u16* __restrict__ wB,
                                                const u16* __restrict__ woB, char* __restrict__ ws,
                                                u16* __restrict__ out) {
  __shared__ __attribute__((aligned(16))) char smem[LDS_BYTES];
  const int tid = threadIdx.x, lane = tid & 63;
  const int w = __builtin_amdgcn_readfirstlane(tid >> 6);
  const int bid = blockIdx.x, xcd = bid & 7, idx = bid >> 3;
  const int h = lane >> 5, blk = (lane >> 4) & 1;
  const int ti = (xcd >> 1) * 4 + (idx & 3), hp = (xcd & 1) * 8 + (idx >> 2);
  const int hl = w >> 2, tt = w & 3;
  const int head = 2 * hp + hl, qt = 4 * ti + tt;
  const int br3 = hp >> 3, nj = hp & 7;
  const __amdgpu_buffer_rsrc_t wsr = __builtin_amdgcn_make_buffer_rsrc(ws, 0, WS_END, 0x00020000);
  const uint32_t sm0 = lds_addr_of(smem);

  STAMP(0);
  const uint32_t cslot = bid * 64 + lane;
  uint32_t pslot = cslot;
  if (lane < 2 && ti + 1 + lane < 16) pslot = bid_of(hp, ti + 1 + lane) * 64 + lane;
  if (lane >= 16 && lane < 32) pslot = bid_of(hp, lane - 16) * 64 + 16 + ti;
  if (lane >= 32 && lane < 48) pslot = bid_of(lane - 32, ti) * 64 + 32 + hp;
  u64 cwant = 0, pval = 0;
  if (w == 0) {
    cwant = __hip_atomic_load((const gu64*)(ws + WS_E) + cslot, RLX_AGENT);
    pval = __hip_atomic_load((const gu64*)(ws + WS_E) + pslot, RLX_AGENT);
  }

  bf16x8 qf, qa, ka, kfb, wf;
  u32x4 va0, va1, vb0, vb1;
  {
    const uint32_t xo0 = (uint32_t)((ti * 128 + 16 * w + (lane >> 3)) * DM + (((lane & 7) ^ ((lane >> 4) & 7)) * 8)) * 2u;
    const uint32_t xo1 = (uint32_t)((ti * 128 + 16 * w + 8 + (lane >> 3)) * DM + (((lane & 7) ^ ((4 + (lane >> 4)) & 7)) * 8)) * 2u;
    const uint32_t wrow = (uint32_t)(16 * tt + (lane >> 2)) * 1536 + hp * 32;
    const uint32_t wo0 = (wrow + hl * 512 + (((lane & 3) ^ (2 * hl)) * 8)) * 2u;
    const uint32_t wo1 = (wrow + 1024 + ((lane & 3) * 8)) * 2u;
    auto stage_load = [&](int s, int buf) {
      const u16* x = (s < 8 ? xB : xA) + (s & 7) * 64;
      const u16* W = (s < 8 ? wB : wA) + (size_t)(s & 7) * 64 * 1536;
      const uint32_t b = sm0 + buf * STAGE_BYTES;
      glds16s(x, xo0, b + w * 2048);
      glds16s(x, xo1, b + w * 2048 + 1024);
      glds16s(W, wo0, b + 16384 + w * 1024);
      if (hl == 0) glds16s(W, wo1, b + 16384 + 8192 + w * 1024);
    };
    if (w == 7) glds16s(wphi + (size_t)hp * 512, (uint32_t)lane * 16u, sm0 + L_EX);
    stage_load(0, 0);
    stage_load(1, 1);
    stage_load(2, 2);

    const int xrow = 32 * tt + (lane & 31);
    const int xa0 = (xrow * 128 + ((h ^ ((xrow >> 1) & 7)) * 16)) ^ (64 * hl);
    const int q4 = (lane & 15) >> 2, p4 = lane & 3;
    const int wrd = 16384 + (32 * hl + 8 * h + q4) * 64 + 8 * (p4 & 1);
    const int wa0 = wrd + blk * 4096 + (((p4 >> 1) ^ (2 * blk)) * 16);
    const int wa1 = wrd + blk * 4096 + (((2 + (p4 >> 1)) ^ (2 * blk)) * 16);
    const int wav = wrd + 8192 + (2 * blk + (p4 >> 1)) * 16;

    f32x16 a0 = zero16(), a1 = zero16(), av = zero16();
    auto exchange = [&](f32x16& keep) {
      char* ex = smem + L_EX + w * EX_SLOT;
      const f32x16 give = hl ? a0 : a1;
      keep = hl ? a1 : a0;
#pragma unroll
      for (int r4 = 0; r4 < 4; ++r4) {
        f32x4 v = {give[4 * r4], give[4 * r4 + 1], give[4 * r4 + 2], give[4 * r4 + 3]};
        *(f32x4*)(ex + r4 * 1024 + lane * 16) = v;
      }
      const int vo = 4096 + (h * 16 + (lane & 15)) * 16;
      if (blk != hl) {
#pragma unroll
        for (int r4 = 0; r4 < 4; ++r4) {
          f32x4 v = {av[4 * r4], av[4 * r4 + 1], av[4 * r4 + 2], av[4 * r4 + 3]};
          *(f32x4*)(ex + vo + r4 * 512) = v;
        }
      }
    };
    auto exchange_read = [&](f32x16& keep) {
      const char* px = smem + L_EX + (w ^ 4) * EX_SLOT;
      const int vo = 4096 + (h * 16 + (lane & 15)) * 16;
#pragma unroll
      for (int r4 = 0; r4 < 4; ++r4) {
        const f32x4 v = *(const f32x4*)(px + r4 * 1024 + lane * 16);
        const f32x4 u = *(const f32x4*)(px + vo + r4 * 512);
#pragma unroll
        for (int c = 0; c < 4; ++c) { keep[4 * r4 + c] += v[c]; av[4 * r4 + c] += u[c]; }
      }
    };

#pragma unroll 1
    for (int s4 = 0; s4 < 4; ++s4) {
#pragma unroll
      for (int u = 0; u < 4; ++u) {
        const int s = 4 * s4 + u;
        if (s4 == 3 && u >= 2) {
          if (u == 2) { if (hl == 0) asm volatile("s_waitcnt vmcnt(4)" ::: "memory"); else asm volatile("s_waitcnt vmcnt(3)" ::: "memory"); }
          else asm volatile("s_waitcnt vmcnt(0)" ::: "memory");
        } else {
          if (hl == 0) asm volatile("s_waitcnt vmcnt(8)" ::: "memory"); else asm volatile("s_waitcnt vmcnt(6)" ::: "memory");
        }
        __builtin_amdgcn_s_barrier();
        asm volatile("" ::: "memory");
        if (s == 0) { STAMP(1); wf = wphi_frag_lds(smem + L_EX + hl * 512, lane); }
        if (s == 10 && w == 0) signal_slots(ws, pslot, mix(pval), lane < 2 && ti + 1 + lane < 16);
        if (s + 3 < 16) stage_load(s + 3, (u + 3) & 3);
        const char* b = smem + u * STAGE_BYTES;
#pragma unroll
        for (int k2 = 0; k2 < 2; ++k2) {
          const bf16x8 xf = lds_read16(b + (xa0 ^ (32 * k2)));
          const bf16x8 f0 = tr_frag(b + wa0 + k2 * 1024, b + wa0 + k2 * 1024 + 256);
          const bf16x8 f1 = tr_frag(b + wa1 + k2 * 1024, b + wa1 + k2 * 1024 + 256);
          const bf16x8 fv = tr_frag(b + wav + k2 * 1024, b + wav + k2 * 1024 + 256);
          a0 = MFMA32(f0, xf, a0);
          a1 = MFMA32(f1, xf, a1);
          av = MFMA32(xf, fv, av);
        }
        asm volatile("s_waitcnt lgkmcnt(0)" ::: "memory");
      }
      if (s4 == 1) {
        STAMP(2);
        f32x16 keep;
        exchange(keep);
        wg_barrier();
        exchange_read(keep);
        qf = pack8s<0>(keep, 0.25f * L2E);
        kfb = pack8<8>(keep);
        vb0 = __builtin_bit_cast(u32x4, pack8<0>(av));
        vb1 = __builtin_bit_cast(u32x4, pack8<8>(av));
        const uint32_t to = (head * NT + qt) * 1024;
        st_sc1_frag(wsr, WS_K + to + lane * 16, kfb);
        if (blk == hl) {
          st_sc1(wsr, WS_V + to + h * 256 + (lane & 15) * 16, vb0);
          st_sc1(wsr, WS_V + to + 512 + h * 256 + (lane & 15) * 16, vb1);
        }
        a0 = zero16(); a1 = zero16(); av = zero16();
        STAMP(3);
      }
    }
    STAMP(4);
    f32x16 keep;
    exchange(keep);
    if (w == 0) {
      cwant = mix(cwant);
      pval = mix(pval);
      wait_slots(ws, cslot, cwant, lane < 2 && ti - 1 - lane >= 0);
    }
    wg_barrier();
    STAMP(5);
    const int t0 = 4 * ti - 8 + 2 * tt;
    u32x4 kv[4];
    if (t0 >= 0) {
      const uint32_t o = (head * NT + t0) * 1024 + lane * 16;
      kv[0] = ld_sc1(wsr, WS_K + o);
      kv[1] = ld_sc1(wsr, WS_K + o + 1024);
      kv[2] = ld_sc1(wsr, WS_V + o);
      kv[3] = ld_sc1(wsr, WS_V + o + 1024);
    }
    exchange_read(keep);
    qa = pack8<0>(keep);
    ka = pack8<8>(keep);
    va0 = __builtin_bit_cast(u32x4, pack8<0>(av));
    va1 = __builtin_bit_cast(u32x4, pack8<8>(av));

    s16x8 i0, i1;
#pragma unroll
    for (int j = 0; j < 8; ++j) {
      const int f = 8 * (j >> 2) + 4 * h + (j & 3);
      i0[j] = ((lane & 31) == f) ? (short)0x3F80 : (short)0;
      i1[j] = ((lane & 31) == 16 + f) ? (short)0x3F80 : (short)0;
    }
    const bf16x8 id0 = __builtin_bit_cast(bf16x8, i0), id1 = __builtin_bit_cast(bf16x8, i1);
    const u32x4 ones = {0x3F803F80u, 0x3F803F80u, 0x3F803F80u, 0x3F803F80u};
    if (blk != hl) { va0 = ones; va1 = ones; }
    f32x16 pk = MFMA32(wf, ka, zero16());
    bf16x8 fp, fn;
    dual_softmax(pk, fp, fn);
    f32x16 T = MFMA32(fp, id0, zero16());
    T = MFMA32(fn, id1, T);
    f32x16 kvp = MFMA32(pack8<0>(T), __builtin_bit_cast(bf16x8, va0), zero16());
    kvp = MFMA32(pack8<8>(T), __builtin_bit_cast(bf16x8, va1), kvp);
    if (blk == hl || (lane & 15) == 0) {
      char* dst = smem + L_EX + (w ^ 4) * EX_SLOT + (blk == hl ? (h * 16 + (lane & 15)) * 16 : 2048 + h * 16);
      const int st = blk == hl ? 512 : 32;
#pragma unroll
      for (int r4 = 0; r4 < 4; ++r4) {
        f32x4 v = {kvp[4 * r4], kvp[4 * r4 + 1], kvp[4 * r4 + 2], kvp[4 * r4 + 3]};
        *(f32x4*)(dst + r4 * st) = v;
      }
    }
    if (w == 4) *(u32x4*)(smem + L_ONES + lane * 16) = ones;
    char* kw = smem + L_K + hl * 12288;
    char* vw = smem + L_V + hl * 12288;
    *(bf16x8*)(kw + (8 + tt) * 1024 + lane * 16) = kfb;
    if (blk == hl) {
      *(u32x4*)(vw + (8 + tt) * 1024 + h * 256 + (lane & 15) * 16) = vb0;
      *(u32x4*)(vw + (8 + tt) * 1024 + 512 + h * 256 + (lane & 15) * 16) = vb1;
    }
    if (t0 >= 0) {
      *(u32x4*)(kw + 2 * tt * 1024 + lane * 16) = kv[0];
      *(u32x4*)(kw + 2 * tt * 1024 + 1024 + lane * 16) = kv[1];
      *(u32x4*)(vw + 2 * tt * 1024 + lane * 16) = kv[2];
      *(u32x4*)(vw + 2 * tt * 1024 + 1024 + lane * 16) = kv[3];
    }
    wg_barrier();
    STAMP(6);
    if (w == 0) {
#pragma unroll
      for (int p = 0; p < 3; ++p) {
        if (p < 2 || lane < 8) {
          const int hh = p < 2 ? p : (lane >> 2);
          const char* src = smem + L_EX + (hh ^ 1) * 4 * EX_SLOT +
                            (p < 2 ? (lane >> 5) * 1024 + (lane & 31) * 16 : 2048 + (lane & 3) * 32);
          const int d2 = p < 2 ? 512 : 16;
          f32x4 s0 = *(const f32x4*)src, s1 = *(const f32x4*)(src + d2);
#pragma unroll
          for (int t = 1; t < 4; ++t) {
            s0 += *(const f32x4*)(src + t * EX_SLOT);
            s1 += *(const f32x4*)(src + t * EX_SLOT + d2);
          }
          bf16x8 o;
#pragma unroll
          for (int c = 0; c < 4; ++c) { o[c] = (__bf16)s0[c]; o[4 + c] = (__bf16)s1[c]; }
          st_sc1_frag(wsr, WS_KVP + ((2 * hp + hh) * 16 + ti) * KVP_BYTES + (p < 2 ? lane * 16 : 1024 + (lane & 3) * 16), o);
        }
      }
    }
  }
  const u16* wo = br3 ? woB : woA;
  auto wo_load = [&]() {
    const uint32_t off = (uint32_t)((64 * w + (lane >> 3)) * DM + nj * 64 + (((lane & 7) ^ (((lane >> 4) & 1) << 2)) * 8)) * 2u;
#pragma unroll
    for (int i = 0; i < 8; ++i) glds16s(wo + i * 8 * DM, off, sm0 + L_WO + w * 8192 + i * 1024);
  };
  if (w != 0) wo_load();
  const bool lower = blk == 0;

  {
    const char* kbase = smem + L_K + hl * 12288 + tt * 1024 + lane * 16;
    const int vinc = lower ? 1024 : 0;
    const char* vbase = lower ? (smem + L_V + hl * 12288 + tt * 1024 + h * 256 + (lane & 15) * 16) : (smem + L_ONES + (lane & 15) * 16);
    const int ql = lane & 31;
    const bool has0 = qt >= 8;
    f32x16 sm = MFMA32(lds_read16(kbase + 8 * 1024), qf, zero16());
    {
      f32x16 s0;
      if (has0) s0 = MFMA32(lds_read16(kbase), qf, zero16());
      else {
#pragma unroll
        for (int r = 0; r < 16; ++r) s0[r] = -INFINITY;
      }
#pragma unroll
      for (int r = 0; r < 16; ++r) {
        const int kl = (r & 3) + 8 * (r >> 2) + 4 * h;
        sm[r] = (kl <= ql) ? sm[r] : s0[r];
      }
    }
    float m = sm[0];
#pragma unroll
    for (int r = 1; r < 16; ++r) m = fmaxf(m, sm[r]);
#pragma unroll
    for (int i = 1; i < 8; ++i) {
      if (qt - 8 + i >= 0) {
        f32x16 s = MFMA32(lds_read16(kbase + i * 1024), qf, zero16());
#pragma unroll
        for (int r = 0; r < 16; r += 2) m = fmaxf(fmaxf(m, s[r]), s[r + 1]);
      }
    }
    m = swap_max(m);
    if (w == 0) {
      asm volatile("s_waitcnt vmcnt(0)" ::: "memory");
      signal_slots(ws, pslot, pval, lane >= 16 && lane < 32);
      wo_load();
    }
    f32x16 negm;
#pragma unroll
    for (int r = 0; r < 16; ++r) negm[r] = -m;
    f32x16 o = zero16();
    {
      f32x16 e;
#pragma unroll
      for (int r = 0; r < 16; ++r) e[r] = __builtin_amdgcn_exp2f(sm[r] - m);
      const u32x4 pa = __builtin_bit_cast(u32x4, pack8<0>(e)), pb = __builtin_bit_cast(u32x4, pack8<8>(e));
      u32x4 da, db, ua, ub;
#pragma unroll
      for (int i = 0; i < 4; ++i) {
        const int ra = 2 * i, rb = 8 + 2 * i;
        const int ka = (ra & 3) + 8 * (ra >> 2) + 4 * h, kb = (rb & 3) + 8 * (rb >> 2) + 4 * h;
        const unsigned ma = (ka <= ql ? 0xFFFFu : 0u) | (ka + 1 <= ql ? 0xFFFF0000u : 0u);
        const unsigned mb = (kb <= ql ? 0xFFFFu : 0u) | (kb + 1 <= ql ? 0xFFFF0000u : 0u);
        da[i] = pa[i] & ma; ua[i] = pa[i] & ~ma;
        db[i] = pb[i] & mb; ub[i] = pb[i] & ~mb;
      }
      const char* vd = vbase + 8 * vinc;
      o = MFMA32(lds_read16(vd), __builtin_bit_cast(bf16x8, da), o);
      o = MFMA32(lds_read16(vd + 512), __builtin_bit_cast(bf16x8, db), o);
      if (has0) {
        o = MFMA32(lds_read16(vbase), __builtin_bit_cast(bf16x8, ua), o);
        o = MFMA32(lds_read16(vbase + 512), __builtin_bit_cast(bf16x8, ub), o);
      }
    }
#pragma unroll
    for (int i = 1; i < 8; ++i) {
      if (qt - 8 + i >= 0) {
        f32x16 s = MFMA32(lds_read16(kbase + i * 1024), qf, negm);
        f32x16 e;
#pragma unroll
        for (int r = 0; r < 16; ++r) e[r] = __builtin_amdgcn_exp2f(s[r]);
        const char* vp = vbase + i * vinc;
        o = MFMA32(lds_read16(vp), pack8<0>(e), o);
        o = MFMA32(lds_read16(vp + 512), pack8<8>(e), o);
      }
    }
    const float inv = __builtin_amdgcn_rcpf(o[8]);
    st_sc1_frag(wsr, WS_O + FRAG_BYTES + (head * NT + qt) * 1024 + lane * 16, pack8s<0>(o, inv));
  }
  STAMP(7);

  {
    if (w == 0) wait_slots(ws, cslot, cwant, lane >= 16 && lane < 32);
    f32x16 pq = MFMA32(wf, qa, zero16());
    bf16x8 fp, fn;
    dual_softmax(pq, fp, fn);
    wg_barrier();
    STAMP(8);
    const uint32_t kb = WS_KVP + (head * 16 + 4 * tt) * KVP_BYTES;
    u32x4 p[4];
#pragma unroll
    for (int i = 0; i < 4; ++i) p[i] = ld_sc1(wsr, kb + i * KVP_BYTES + lane * 16);
    const u32x4 pz = ld_sc1(wsr, kb + ((lane >> 2) & 3) * KVP_BYTES + 1024 + (lane & 3) * 16);
    f32x4 s0 = {0.f, 0.f, 0.f, 0.f}, s1 = s0, z0, z1;
#pragma unroll
    for (int i = 0; i < 4; ++i) {
      s0[0] += __uint_as_float(p[i][0] << 16); s0[1] += __uint_as_float(p[i][0] & 0xFFFF0000u);
      s0[2] += __uint_as_float(p[i][1] << 16); s0[3] += __uint_as_float(p[i][1] & 0xFFFF0000u);
      s1[0] += __uint_as_float(p[i][2] << 16); s1[1] += __uint_as_float(p[i][2] & 0xFFFF0000u);
      s1[2] += __uint_as_float(p[i][3] << 16); s1[3] += __uint_as_float(p[i][3] & 0xFFFF0000u);
    }
    z0[0] = __uint_as_float(pz[0] << 16); z0[1] = __uint_as_float(pz[0] & 0xFFFF0000u);
    z0[2] = __uint_as_float(pz[1] << 16); z0[3] = __uint_as_float(pz[1] & 0xFFFF0000u);
    z1[0] = __uint_as_float(pz[2] << 16); z1[1] = __uint_as_float(pz[2] & 0xFFFF0000u);
    z1[2] = __uint_as_float(pz[3] << 16); z1[3] = __uint_as_float(pz[3] & 0xFFFF0000u);
#pragma unroll
    for (int c = 0; c < 4; ++c) {
      z0[c] += __shfl_xor(z0[c], 4); z0[c] += __shfl_xor(z0[c], 8);
      z1[c] += __shfl_xor(z1[c], 4); z1[c] += __shfl_xor(z1[c], 8);
    }
    *(f32x4*)(smem + L_RED2 + w * 2048 + lane * 16) = s0;
    *(f32x4*)(smem + L_RED2 + w * 2048 + 1024 + lane * 16) = s1;
    if (lane < 4) {
      *(f32x4*)(smem + L_ZRED + w * 128 + lane * 32) = z0;
      *(f32x4*)(smem + L_ZRED + w * 128 + lane * 32 + 16) = z1;
    }
    wg_barrier();
    if (tt == 0) {
      const char* src = smem + L_RED2 + w * 2048 + lane * 16;
      f32x4 t0 = *(const f32x4*)src, t1 = *(const f32x4*)(src + 1024);
#pragma unroll
      for (int t = 1; t < 4; ++t) { t0 += *(const f32x4*)(src + t * 2048); t1 += *(const f32x4*)(src + t * 2048 + 1024); }
      bf16x8 o;
#pragma unroll
      for (int c = 0; c < 4; ++c) { o[c] = (__bf16)t0[c]; o[4 + c] = (__bf16)t1[c]; }
      *(bf16x8*)(smem + L_KVF + hl * 2048 + (lane >> 5) * 1024 + (((lane >> 4) & 1) * 32 + (lane & 15)) * 16) = o;
    }
    if (tt == 1) {
      const int f = lane & 31;
      const float* zr = (const float*)(smem + L_ZRED + hl * 512);
      const __bf16 z = (__bf16)((zr[f] + zr[32 + f]) + (zr[64 + f] + zr[96 + f]));
#pragma unroll
      for (int i = 0; i < 8; ++i)
        *(__bf16*)(smem + L_KVF + hl * 2048 + (f >> 4) * 1024 + (((f >> 2) & 1) * 32 + 16 + 8 * h + i) * 16 + (((f >> 3) & 1) * 4 + (f & 3)) * 2) = z;
    }
    wg_barrier();
    f32x16 o = MFMA32(lds_read16(smem + L_KVF + hl * 2048 + lane * 16), fp, zero16());
    o = MFMA32(lds_read16(smem + L_KVF + hl * 2048 + 1024 + lane * 16), fn, o);
    const float inv = __builtin_amdgcn_rcpf(o[8] + 1e-6f);
    st_sc1_frag(wsr, WS_O + (head * NT + qt) * 1024 + lane * 16, pack8s<0>(o, inv));
    STAMP(9);
    drain_barrier();
    STAMP(10);
    if (w == 0) {
      signal_slots(ws, pslot, pval, lane >= 32 && lane < 48);
      wait_slots(ws, cslot, cwant, lane >= 32 && lane < 48);
    }
    wg_barrier();
    STAMP(11);
  }

  {
    const int kh = hl;
    const int tile32 = qt;
    const uint32_t og = WS_O + br3 * FRAG_BYTES + ((16 * kh) * NT + tile32) * 1024 + lane * 16;
    bf16x8 of[16];
#pragma unroll
    for (int i = 0; i < 16; ++i) of[i] = ld_sc1_frag(wsr, og + i * NT * 1024);
    const int q4 = (lane & 15) >> 2, p4 = lane & 3;
    f32x16 acc0 = zero16(), acc1 = zero16();
    const char* wl = smem + L_WO;
    const int ra0 = (256 * kh + 4 * h + q4) * 128 + (((2 * blk + (p4 >> 1)) ^ (((q4 >> 1) & 1) << 2)) * 16) + 8 * (p4 & 1);
    const int ra1 = (256 * kh + 4 * h + q4) * 128 + (((4 + 2 * blk + (p4 >> 1)) ^ (((q4 >> 1) & 1) << 2)) * 16) + 8 * (p4 & 1);
#pragma unroll
    for (int i = 0; i < 16; ++i) {
      const int kb = 16 * i * 128;
      const bf16x8 a0 = tr_frag(wl + ra0 + kb, wl + ra0 + kb + 1024);
      const bf16x8 a1 = tr_frag(wl + ra1 + kb, wl + ra1 + kb + 1024);
      acc0 = MFMA32(a0, of[i], acc0);
      acc1 = MFMA32(a1, of[i], acc1);
    }
    STAMP(12);
    float* ex = (float*)(smem + L_K) + w * 1024 + lane * 4;
    const f32x16 give = kh ? acc0 : acc1;
    f32x16 keep = kh ? acc1 : acc0;
#pragma unroll
    for (int r4 = 0; r4 < 4; ++r4) {
      f32x4 v = {give[4 * r4], give[4 * r4 + 1], give[4 * r4 + 2], give[4 * r4 + 3]};
      *(f32x4*)(ex + r4 * 256) = v;
    }
    wg_barrier();
    const float* px = (const float*)(smem + L_K) + (w ^ 4) * 1024 + lane * 4;
#pragma unroll
    for (int r4 = 0; r4 < 4; ++r4) {
      const f32x4 v = *(const f32x4*)(px + r4 * 256);
#pragma unroll
      for (int c = 0; c < 4; ++c) keep[4 * r4 + c] += v[c];
    }
    const u32x4 lo = __builtin_bit_cast(u32x4, pack8<0>(keep)), hi = __builtin_bit_cast(u32x4, pack8<8>(keep));
    u16* op = out + (size_t)(tile32 * 32 + (lane & 31)) * 1024 + br3 * 512 + nj * 64 + kh * 32 + h * 8;
    auto t0 = __builtin_amdgcn_permlane32_swap(lo[0], lo[2], false, false);
    auto t1 = __builtin_amdgcn_permlane32_swap(lo[1], lo[3], false, false);
    u32x4 v = {t0[0], t1[0], t0[1], t1[1]};
    *(u32x4*)op = v;
    auto t2 = __builtin_amdgcn_permlane32_swap(hi[0], hi[2], false, false);
    auto t3 = __builtin_amdgcn_permlane32_swap(hi[1], hi[3], false, false);
    u32x4 u = {t2[0], t3[0], t2[1], t3[1]};
    *(u32x4*)(op + 16) = u;
  }
  STAMP(13);
}

extern "C" void kernel_launch(void* const* d_in, const int* in_sizes, int n_in, void* d_out, int out_size,
                              void* d_ws, size_t ws_size, hipStream_t stream) {
  fused<<<256, 512, 0, stream>>>((const u16*)d_in[0], (const u16*)d_in[1], (const u16*)d_in[2], (const u16*)d_in[3],
                                 (const u16*)d_in[4], (const u16*)d_in[5], (const u16*)d_in[6], (char*)d_ws, (u16*)d_out);
}
```

```cpp
#include <hip/hip_runtime.h>
#include <hip/hip_bf16.h>
#include <stdint.h>

typedef __bf16 bf16x8 __attribute__((ext_vector_type(8)));
typedef __bf16 bf16x4 __attribute__((ext_vector_type(4)));
typedef short s16x4 __attribute__((ext_vector_type(4)));
typedef short s16x8 __attribute__((ext_vector_type(8)));
typedef float f32x16 __attribute__((ext_vector_type(16)));
typedef float f32x4 __attribute__((ext_vector_type(4)));
typedef unsigned u32x4 __attribute__((ext_vector_type(4)));
typedef unsigned u32x2 __attribute__((ext_vector_type(2)));
typedef unsigned short u16;

#define LDS_AS __attribute__((address_space(3)))
#define GLB_AS __attribute__((address_space(1)))
#define DEVI __device__ __forceinline__
#define MFMA32(a, b, c) __builtin_amdgcn_mfma_f32_32x32x16_bf16((a), (b), (c), 0, 0, 0)

#define STAMP(i)
constexpr int SEQ = 2048, DM = 512, NH = 32, NT = 64;
constexpr float L2E = 1.4426950408889634f;

constexpr uint32_t FRAG_BYTES = NH * NT * 1024;
constexpr uint32_t WS_QA = 0, WS_QB = FRAG_BYTES, WS_KB = 2 * FRAG_BYTES, WS_VB = 3 * FRAG_BYTES;
constexpr uint32_t WS_O = 4 * FRAG_BYTES;
constexpr uint32_t WS_KVP = 6 * FRAG_BYTES;
constexpr uint32_t KVP_BYTES = 1088;
constexpr uint32_t WS_E = WS_KVP + NH * 16 * KVP_BYTES;
constexpr uint32_t WS_F = WS_E + 256 * 64 * 8;
constexpr uint32_t WS_END = WS_F + 256 * 64 * 8;

DEVI void glds16s(const void* sbase, uint32_t voff, uint32_t lds_addr) {
  asm volatile("s_mov_b32 m0, %2\n\ts_nop 0\n\tglobal_load_lds_dwordx4 %0, %1" ::"v"(voff), "s"(sbase), "s"(lds_addr) : "memory");
}
DEVI uint32_t lds_addr_of(const char* p) { return (uint32_t)(uintptr_t)(const LDS_AS char*)p; }
DEVI bf16x8 lds_read16(const char* p) { return *(const bf16x8*)p; }
DEVI bf16x8 tr_frag(const char* p0, const char* p1) {
  s16x4 a = __builtin_amdgcn_ds_read_tr16_b64_v4i16((LDS_AS s16x4*)p0);
  s16x4 b = __builtin_amdgcn_ds_read_tr16_b64_v4i16((LDS_AS s16x4*)p1);
  s16x8 r = __builtin_shufflevector(a, b, 0, 1, 2, 3, 4, 5, 6, 7);
  return __builtin_bit_cast(bf16x8, r);
}
DEVI float swap_max(float x) {
  auto t = __builtin_amdgcn_permlane32_swap(__float_as_uint(x), __float_as_uint(x), false, false);
  return fmaxf(__uint_as_float(t[0]), __uint_as_float(t[1]));
}
DEVI float swap_add(float x) {
  auto t = __builtin_amdgcn_permlane32_swap(__float_as_uint(x), __float_as_uint(x), false, false);
  return __uint_as_float(t[0]) + __uint_as_float(t[1]);
}
template <int O>
DEVI bf16x8 pack8(const f32x16& a) {
  bf16x8 r;
#pragma unroll
  for (int j = 0; j < 8; ++j) r[j] = (__bf16)a[O + j];
  return r;
}
template <int O>
DEVI bf16x8 pack8s(const f32x16& a, float s) {
  bf16x8 r;
#pragma unroll
  for (int j = 0; j < 8; ++j) r[j] = (__bf16)(a[O + j] * s);
  return r;
}
DEVI f32x16 zero16() {
  f32x16 z;
#pragma unroll
  for (int i = 0; i < 16; ++i) z[i] = 0.f;
  return z;
}
DEVI bf16x8 wphi_frag_lds(const char* wl, int lane) {
  const char* p = wl + (4 * (lane >> 5) + ((lane & 15) >> 2)) * 32 + (lane & 3) * 8;
  const u32x4 r = __builtin_bit_cast(u32x4, tr_frag(p, p + 256));
  const unsigned sgn = (lane & 16) ? 0x80008000u : 0u;
  const u32x4 o = {r[0] ^ sgn, r[1] ^ sgn, r[2] ^ sgn, r[3] ^ sgn};
  return __builtin_bit_cast(bf16x8, o);
}
DEVI void dual_softmax(const f32x16& p, bf16x8& fp, bf16x8& fn) {
  float mp = fmaxf(fmaxf(fmaxf(p[0], p[1]), fmaxf(p[2], p[3])), fmaxf(fmaxf(p[4], p[5]), fmaxf(p[6], p[7])));
  float mn = fmaxf(fmaxf(fmaxf(p[8], p[9]), fmaxf(p[10], p[11])), fmaxf(fmaxf(p[12], p[13]), fmaxf(p[14], p[15])));
  mp = swap_max(mp) * L2E;
  mn = swap_max(mn) * L2E;
  f32x16 e;
  float sp = 0.f, sn = 0.f;
#pragma unroll
  for (int j = 0; j < 8; ++j) {
    e[j] = __builtin_amdgcn_exp2f(__builtin_fmaf(p[j], L2E, -mp));
    e[8 + j] = __builtin_amdgcn_exp2f(__builtin_fmaf(p[8 + j], L2E, -mn));
    sp += e[j];
    sn += e[8 + j];
  }
  sp = __builtin_amdgcn_rcpf(swap_add(sp));
  sn = __builtin_amdgcn_rcpf(swap_add(sn));
  fp = pack8s<0>(e, sp);
  fn = pack8s<8>(e, sn);
}

typedef unsigned long long u64;
typedef GLB_AS u64 gu64;
#define RLX_AGENT __ATOMIC_RELAXED, __HIP_MEMORY_SCOPE_AGENT
constexpr int AUX_SC1 = 16;
DEVI u32x4 ld_sc1(__amdgpu_buffer_rsrc_t r, uint32_t off) { return __builtin_amdgcn_raw_buffer_load_b128(r, off, 0, AUX_SC1); }
DEVI bf16x8 ld_sc1_frag(__amdgpu_buffer_rsrc_t r, uint32_t off) { return __builtin_bit_cast(bf16x8, ld_sc1(r, off)); }
DEVI void st_sc1(__amdgpu_buffer_rsrc_t r, uint32_t off, u32x4 v) { __builtin_amdgcn_raw_buffer_store_b128(v, r, off, 0, AUX_SC1); }
DEVI void st_sc1_frag(__amdgpu_buffer_rsrc_t r, uint32_t off, bf16x8 v) { st_sc1(r, off, __builtin_bit_cast(u32x4, v)); }
DEVI void wg_barrier() {
  asm volatile("s_waitcnt lgkmcnt(0)" ::: "memory");
  __builtin_amdgcn_s_barrier();
  asm volatile("" ::: "memory");
}
DEVI void drain_barrier() {
  asm volatile("s_waitcnt vmcnt(0) lgkmcnt(0)" ::: "memory");
  __builtin_amdgcn_s_barrier();
  asm volatile("" ::: "memory");
}
DEVI u64 mix(u64 e) { return e * 0x9E3779B97F4A7C15ull + 0x632BE59BD9B4E019ull; }
DEVI void wait_slots(char* ws, uint32_t slot, u64 want, bool active) {
  const gu64* f = (const gu64*)(ws + WS_F) + slot;
  for (int it = 0; it < 200000; ++it) {
    const u64 v = active ? __hip_atomic_load(f, RLX_AGENT) : want;
    if (__builtin_amdgcn_ballot_w64(v != want) == 0) break;
    __builtin_amdgcn_s_sleep(1);
  }
  if (active) __hip_atomic_store((gu64*)(ws + WS_E) + slot, want, RLX_AGENT);
}
DEVI void spin_slots(char* ws, uint32_t slot, u64 want, bool active) {
  const gu64* f = (const gu64*)(ws + WS_F) + slot;
  for (int it = 0; it < 200000; ++it) {
    const u64 v = active ? __hip_atomic_load(f, RLX_AGENT) : want;
    if (__builtin_amdgcn_ballot_w64(v != want) == 0) break;
    __builtin_amdgcn_s_sleep(1);
  }
}
DEVI void signal_slots(char* ws, uint32_t slot, u64 val, bool active) {
  if (active) __hip_atomic_store((gu64*)(ws + WS_F) + slot, val, RLX_AGENT);
}

constexpr int STAGE_BYTES = 40960;
constexpr int L_WPHI = 3 * STAGE_BYTES;
constexpr int L_K = 0, L_V = 16384, L_ONES = 32768, L_RED = 33792, L_ZRED = L_RED + 8192, L_KVF = L_ZRED + 512, L_WO = 57344;
constexpr int L_WPHI2 = L_WPHI + 2048;
constexpr int LDS_BYTES = L_WPHI2 + 1024;

__global__ __launch_bounds__(512, 2) void fused(const u16* __restrict__ xA, const u16* __restrict__ wA,
                                                const u16* __restrict__ wphi, const u16* __restrict__ woA,
                                                const u16* __restrict__ xB, const u16* __restrict__ wB,
                                                const u16* __restrict__ woB, char* __restrict__ ws,
                                                u16* __restrict__ out) {
  __shared__ __attribute__((aligned(16))) char smem[LDS_BYTES];
  const int tid = threadIdx.x, lane = tid & 63;
  const int w = __builtin_amdgcn_readfirstlane(tid >> 6);
  const int bid = blockIdx.x, xcd = bid & 7, idx = bid >> 3;
  const int h = lane >> 5;
  const int br1 = xcd >> 2, jx = xcd & 3;
  const int ti1 = (jx & 1) * 8 + (idx & 7);
  const int g1 = (jx >> 1) * 4 + (idx >> 3);
  const int head = bid & 31, chunk = bid >> 5;
  const int br3 = xcd >> 2, ti3 = (xcd & 3) * 4 + (idx & 3), nj = idx >> 2;
  const __amdgpu_buffer_rsrc_t wsr = __builtin_amdgcn_make_buffer_rsrc(ws, 0, WS_END, 0x00020000);
  const uint32_t sm0 = lds_addr_of(smem);

  STAMP(0);
  const uint32_t cslot = bid * 64 + lane;
  uint32_t pslot;
  bool pact;
  {
    const int cc = (ti1 >> 1) + (lane >> 2);
    const int j = lane - 32, tj = 2 * chunk + ((j >> 3) & 1);
    const int c3 = ((j & 7) * 4 + (tj & 3)) * 8 + (j >> 4) * 4 + (tj >> 2);
    if (lane >= 32) { pact = true; pslot = c3 * 64 + 32 + head; }
    else if (br1) { pact = lane < 8 && cc <= 7; pslot = (cc * 32 + 4 * g1 + (lane & 3)) * 64 + (ti1 - 2 * cc + 2); }
    else { pact = true; pslot = ((lane >> 2) * 32 + 4 * g1 + (lane & 3)) * 64 + 16 + ti1; }
    if (!pact) pslot = cslot;
  }
  u64 cwant = 0, pval = 0;
  cwant = __hip_atomic_load((const gu64*)(ws + WS_E) + cslot, RLX_AGENT);
  if (w == 0) pval = __hip_atomic_load((const gu64*)(ws + WS_E) + pslot, RLX_AGENT);

  {
    const u16* x = br1 ? xB : xA;
    const u16* W = br1 ? wB : wA;
    const int tt = w & 3, hp = w >> 2;
    const uint32_t xo0 = (uint32_t)((ti1 * 128 + 16 * w + (lane >> 3)) * DM + (((lane & 7) ^ ((lane >> 4) & 7)) * 8)) * 2u;
    const uint32_t xo1 = (uint32_t)((ti1 * 128 + 16 * w + 8 + (lane >> 3)) * DM + (((lane & 7) ^ ((4 + (lane >> 4)) & 7)) * 8)) * 2u;
    const uint32_t wo0 = (uint32_t)((8 * w + (lane >> 3)) * 1536 + g1 * 64 + (((lane & 7) ^ (((lane >> 4) & 1) << 2)) * 8)) * 2u;
    const int s0 = ((idx & 7) + 2 * (idx >> 3)) & 7;
    auto stage_load = [&](int su, int buf) {
      const int s = (su + s0) & 7;
      const uint32_t b = sm0 + buf * STAGE_BYTES;
      glds16s(x + s * 64, xo0, b + w * 2048);
      glds16s(x + s * 64, xo1, b + w * 2048 + 1024);
      glds16s(W + (size_t)s * 64 * 1536, wo0, b + 16384 + w * 1024);
      glds16s(W + (size_t)s * 64 * 1536 + 512, wo0, b + 16384 + 8192 + w * 1024);
      glds16s(W + (size_t)s * 64 * 1536 + 1024, wo0, b + 16384 + 16384 + w * 1024);
    };
    if (w == 1) glds16s(wphi + (size_t)(head & ~1) * 256, (uint32_t)lane * 16u, sm0 + L_WPHI2);
    if (br1 == 0 && w >= 6) glds16s(wphi + (size_t)(4 * g1) * 256 + (w - 6) * 512, (uint32_t)lane * 16u, sm0 + L_WPHI + (w - 6) * 1024);
    stage_load(0, 0);
    stage_load(1, 1);

    const int xrow = 32 * tt + (lane & 31);
    const int xa0 = xrow * 128 + ((h ^ ((xrow >> 1) & 7)) * 16);
    const int q4 = (lane & 15) >> 2, p4 = lane & 3;
    const int wa0 = 16384 + (8 * h + q4) * 128 + (((4 * hp + 2 * ((lane >> 4) & 1) + (p4 >> 1)) ^ (((q4 >> 1) & 1) << 2)) * 16) + 8 * (p4 & 1);

    f32x16 aq = zero16(), ak = zero16(), av = zero16();
#pragma unroll
    for (int s = 0; s < 8; ++s) {
      if (s < 7) asm volatile("s_waitcnt vmcnt(5)" ::: "memory");
      else asm volatile("s_waitcnt vmcnt(0)" ::: "memory");
      __builtin_amdgcn_s_barrier();
      asm volatile("" ::: "memory");
      if (s == 0) STAMP(1);
      if (s + 2 < 8) stage_load(s + 2, (s + 2) % 3);
      const char* b = smem + (s % 3) * STAGE_BYTES;
#pragma unroll
      for (int ks = 0; ks < 4; ++ks) {
        bf16x8 xf = lds_read16(b + (xa0 ^ (32 * ks)));
        const char* wb = b + wa0 + ks * 2048;
        bf16x8 fq = tr_frag(wb, wb + 512);
        bf16x8 fk = tr_frag(wb + 8192, wb + 8192 + 512);
        bf16x8 fv = tr_frag(wb + 16384, wb + 16384 + 512);
        aq = MFMA32(fq, xf, aq);
        ak = MFMA32(fk, xf, ak);
        av = MFMA32(xf, fv, av);
      }
      asm volatile("s_waitcnt lgkmcnt(0)" ::: "memory");
    }
    STAMP(2);
    const int tile32 = ti1 * 4 + tt, head0 = 4 * g1 + 2 * hp, blk = (lane >> 4) & 1;
    if (br1) {
      const uint32_t qo = (head0 * NT + tile32) * 1024 + lane * 16;
      st_sc1_frag(wsr, WS_QB + qo, pack8s<0>(aq, 0.25f * L2E));
      st_sc1_frag(wsr, WS_QB + qo + NT * 1024, pack8s<8>(aq, 0.25f * L2E));
      st_sc1_frag(wsr, WS_KB + qo, pack8<0>(ak));
      st_sc1_frag(wsr, WS_KB + qo + NT * 1024, pack8<8>(ak));
      const uint32_t vo = WS_VB + ((head0 + ((lane >> 4) & 1)) * NT + tile32) * 1024 + h * 256 + (lane & 15) * 16;
      st_sc1_frag(wsr, vo, pack8<0>(av));
      st_sc1_frag(wsr, vo + 512, pack8<8>(av));
    } else {
      const uint32_t qo = WS_QA + (head0 * NT + tile32) * 1024 + lane * 16;
      st_sc1_frag(wsr, qo, pack8<0>(aq));
      st_sc1_frag(wsr, qo + NT * 1024, pack8<8>(aq));
      s16x8 i0, i1;
#pragma unroll
      for (int j = 0; j < 8; ++j) {
        const int f = 8 * (j >> 2) + 4 * h + (j & 3);
        i0[j] = ((lane & 31) == f) ? (short)0x3F80 : (short)0;
        i1[j] = ((lane & 31) == 16 + f) ? (short)0x3F80 : (short)0;
      }
      const bf16x8 id0 = __builtin_bit_cast(bf16x8, i0), id1 = __builtin_bit_cast(bf16x8, i1);
      const u32x4 ones = {0x3F803F80u, 0x3F803F80u, 0x3F803F80u, 0x3F803F80u};
      const u32x4 vf0 = __builtin_bit_cast(u32x4, pack8<0>(av)), vf1 = __builtin_bit_cast(u32x4, pack8<8>(av));
      f32x16 pk0 = MFMA32(wphi_frag_lds(smem + L_WPHI + (2 * hp) * 512, lane), pack8<0>(ak), zero16());
      f32x16 pk1 = MFMA32(wphi_frag_lds(smem + L_WPHI + (2 * hp + 1) * 512, lane), pack8<8>(ak), zero16());
      bf16x8 fp[2], fn[2];
      dual_softmax(pk0, fp[0], fn[0]);
      dual_softmax(pk1, fp[1], fn[1]);
#pragma unroll
      for (int p = 0; p < 2; ++p) {
        f32x16 T = MFMA32(fp[p], id0, zero16());
        T = MFMA32(fn[p], id1, T);
        const bool mine = blk == p;
        f32x16 kvp = MFMA32(pack8<0>(T), __builtin_bit_cast(bf16x8, mine ? vf0 : ones), zero16());
        kvp = MFMA32(pack8<8>(T), __builtin_bit_cast(bf16x8, mine ? vf1 : ones), kvp);
        if (mine || (lane & 15) == 0) {
          char* dst = smem + ((2 * hp + p) * 4 + tt) * 2176 + (mine ? (h * 16 + (lane & 15)) * 16 : 2048 + h * 16);
          const int st = mine ? 512 : 32;
#pragma unroll
          for (int r4 = 0; r4 < 4; ++r4) {
            f32x4 v = {kvp[4 * r4], kvp[4 * r4 + 1], kvp[4 * r4 + 2], kvp[4 * r4 + 3]};
            *(f32x4*)(dst + r4 * st) = v;
          }
        }
      }
      wg_barrier();
      if (w < 4 || (w == 4 && lane < 16)) {
        const int hh = w < 4 ? w : (lane >> 2);
        const char* src = smem + hh * 4 * 2176 + (w < 4 ? (lane >> 5) * 1024 + (lane & 31) * 16 : 2048 + (lane & 3) * 32);
        const int d2 = w < 4 ? 512 : 16;
        f32x4 s0v = *(const f32x4*)src, s1v = *(const f32x4*)(src + d2);
#pragma unroll
        for (int t = 1; t < 4; ++t) {
          s0v += *(const f32x4*)(src + t * 2176);
          s1v += *(const f32x4*)(src + t * 2176 + d2);
        }
        bf16x8 o;
#pragma unroll
        for (int c = 0; c < 4; ++c) { o[c] = (__bf16)s0v[c]; o[4 + c] = (__bf16)s1v[c]; }
        st_sc1_frag(wsr, WS_KVP + ((4 * g1 + hh) * 16 + ti1) * KVP_BYTES + (w < 4 ? lane * 16 : 1024 + (lane & 3) * 16), o);
      }
    }
    drain_barrier();
    STAMP(3);
    cwant = mix(cwant);
    if (w == 0) {
      pval = mix(pval);
      signal_slots(ws, pslot, pval, pact && lane < 32);
      wait_slots(ws, cslot, cwant, lane < 4 && 2 * chunk - 2 + lane >= 0);
    }
    if (tid >= 448) {
      const u32x4 ones = {0x3F803F80u, 0x3F803F80u, 0x3F803F80u, 0x3F803F80u};
      *(u32x4*)(smem + L_ONES + (tid - 448) * 16) = ones;
    }
    wg_barrier();
    STAMP(4);
  }

  const int qt = chunk * 8 + w;
  const bool lower = (lane & 16) == 0;
  bf16x8 qf;
  {
    const int t0 = chunk * 8 - 8;
    u32x4 kv[4];
    if (t0 + 2 * w >= 0) {
      const uint32_t o = (head * NT + t0 + 2 * w) * 1024 + lane * 16;
      kv[0] = ld_sc1(wsr, WS_KB + o);
      kv[1] = ld_sc1(wsr, WS_KB + o + 1024);
      kv[2] = ld_sc1(wsr, WS_VB + o);
      kv[3] = ld_sc1(wsr, WS_VB + o + 1024);
    }
    qf = ld_sc1_frag(wsr, WS_QB + (head * NT + qt) * 1024 + lane * 16);
    if (t0 + 2 * w >= 0) {
      *(u32x4*)(smem + L_K + 2 * w * 1024 + lane * 16) = kv[0];
      *(u32x4*)(smem + L_K + 2 * w * 1024 + 1024 + lane * 16) = kv[1];
      *(u32x4*)(smem + L_V + 2 * w * 1024 + lane * 16) = kv[2];
      *(u32x4*)(smem + L_V + 2 * w * 1024 + 1024 + lane * 16) = kv[3];
    }
    wg_barrier();
    STAMP(5);
  }
  const bool aslot = lane >= 16 && lane < 32;
  const u64 early = aslot ? __hip_atomic_load((const gu64*)(ws + WS_F) + cslot, RLX_AGENT) : cwant;
  u32x4 pp[2], pz;
  bf16x8 qa;
  const u16* wo = br3 ? woB : woA;
  auto wo_load = [&]() {
    const uint32_t off = (uint32_t)((64 * w + (lane >> 3)) * DM + nj * 64 + (((lane & 7) ^ (((lane >> 4) & 1) << 2)) * 8)) * 2u;
#pragma unroll
    for (int i = 0; i < 8; ++i) glds16s(wo + i * 8 * DM, off, sm0 + L_WO + w * 8192 + i * 1024);
  };

  {
    const char* kbase = smem + L_K + w * 1024 + lane * 16;
    const int vinc = lower ? 1024 : 0;
    const char* vbase = lower ? (smem + L_V + w * 1024 + h * 256 + (lane & 15) * 16) : (smem + L_ONES + (lane & 15) * 16);
    const int ql = lane & 31;
    const bool has0 = qt >= 8;
    f32x16 sm = MFMA32(lds_read16(kbase + 8 * 1024), qf, zero16());
    {
      f32x16 s0;
      if (has0) s0 = MFMA32(lds_read16(kbase), qf, zero16());
      else {
#pragma unroll
        for (int r = 0; r < 16; ++r) s0[r] = -INFINITY;
      }
#pragma unroll
      for (int r = 0; r < 16; ++r) {
        const int kl = (r & 3) + 8 * (r >> 2) + 4 * h;
        sm[r] = (kl <= ql) ? sm[r] : s0[r];
      }
    }
    float m = sm[0];
#pragma unroll
    for (int r = 1; r < 16; ++r) m = fmaxf(m, sm[r]);
#pragma unroll
    for (int i = 1; i < 8; ++i) {
      if (qt - 8 + i >= 0) {
        f32x16 s = MFMA32(lds_read16(kbase + i * 1024), qf, zero16());
#pragma unroll
        for (int r = 0; r < 16; r += 2) m = fmaxf(fmaxf(m, s[r]), s[r + 1]);
      }
    }
    m = swap_max(m);
    if (__builtin_amdgcn_ballot_w64(early != cwant) != 0) spin_slots(ws, cslot, cwant, aslot);
    asm volatile("" ::: "memory");
    if (w == 0 && aslot) __hip_atomic_store((gu64*)(ws + WS_E) + cslot, cwant, RLX_AGENT);
    {
      const uint32_t kb = WS_KVP + (head * 16 + 2 * w) * KVP_BYTES;
      pp[0] = ld_sc1(wsr, kb + lane * 16);
      pp[1] = ld_sc1(wsr, kb + KVP_BYTES + lane * 16);
      pz = ld_sc1(wsr, kb + ((lane >> 2) & 1) * KVP_BYTES + 1024 + (lane & 3) * 16);
      qa = ld_sc1_frag(wsr, WS_QA + (head * NT + qt) * 1024 + lane * 16);
    }
    wo_load();
    f32x16 negm;
#pragma unroll
    for (int r = 0; r < 16; ++r) negm[r] = -m;
    f32x16 o = zero16();
    {
      f32x16 e;
#pragma unroll
      for (int r = 0; r < 16; ++r) e[r] = __builtin_amdgcn_exp2f(sm[r] - m);
      const u32x4 pa = __builtin_bit_cast(u32x4, pack8<0>(e)), pb = __builtin_bit_cast(u32x4, pack8<8>(e));
      u32x4 da, db, ua, ub;
#pragma unroll
      for (int i = 0; i < 4; ++i) {
        const int ra = 2 * i, rb = 8 + 2 * i;
        const int ka = (ra & 3) + 8 * (ra >> 2) + 4 * h, kb = (rb & 3) + 8 * (rb >> 2) + 4 * h;
        const unsigned ma = (ka <= ql ? 0xFFFFu : 0u) | (ka + 1 <= ql ? 0xFFFF0000u : 0u);
        const unsigned mb = (kb <= ql ? 0xFFFFu : 0u) | (kb + 1 <= ql ? 0xFFFF0000u : 0u);
        da[i] = pa[i] & ma; ua[i] = pa[i] & ~ma;
        db[i] = pb[i] & mb; ub[i] = pb[i] & ~mb;
      }
      const char* vd = vbase + 8 * vinc;
      o = MFMA32(lds_read16(vd), __builtin_bit_cast(bf16x8, da), o);
      o = MFMA32(lds_read16(vd + 512), __builtin_bit_cast(bf16x8, db), o);
      if (has0) {
        o = MFMA32(lds_read16(vbase), __builtin_bit_cast(bf16x8, ua), o);
        o = MFMA32(lds_read16(vbase + 512), __builtin_bit_cast(bf16x8, ub), o);
      }
    }
#pragma unroll
    for (int i = 1; i < 8; ++i) {
      if (qt - 8 + i >= 0) {
        f32x16 s = MFMA32(lds_read16(kbase + i * 1024), qf, negm);
        f32x16 e;
#pragma unroll
        for (int r = 0; r < 16; ++r) e[r] = __builtin_amdgcn_exp2f(s[r]);
        const char* vp = vbase + i * vinc;
        o = MFMA32(lds_read16(vp), pack8<0>(e), o);
        o = MFMA32(lds_read16(vp + 512), pack8<8>(e), o);
      }
    }
    const float inv = __builtin_amdgcn_rcpf(o[8]);
    st_sc1_frag(wsr, WS_O + FRAG_BYTES + (head * NT + qt) * 1024 + lane * 16, pack8s<0>(o, inv));
  }
  STAMP(6);

  {
    f32x4 s0v, s1v, z0, z1;
    s0v[0] = __uint_as_float(pp[0][0] << 16) + __uint_as_float(pp[1][0] << 16);
    s0v[1] = __uint_as_float(pp[0][0] & 0xFFFF0000u) + __uint_as_float(pp[1][0] & 0xFFFF0000u);
    s0v[2] = __uint_as_float(pp[0][1] << 16) + __uint_as_float(pp[1][1] << 16);
    s0v[3] = __uint_as_float(pp[0][1] & 0xFFFF0000u) + __uint_as_float(pp[1][1] & 0xFFFF0000u);
    s1v[0] = __uint_as_float(pp[0][2] << 16) + __uint_as_float(pp[1][2] << 16);
    s1v[1] = __uint_as_float(pp[0][2] & 0xFFFF0000u) + __uint_as_float(pp[1][2] & 0xFFFF0000u);
    s1v[2] = __uint_as_float(pp[0][3] << 16) + __uint_as_float(pp[1][3] << 16);
    s1v[3] = __uint_as_float(pp[0][3] & 0xFFFF0000u) + __uint_as_float(pp[1][3] & 0xFFFF0000u);
    z0[0] = __uint_as_float(pz[0] << 16); z0[1] = __uint_as_float(pz[0] & 0xFFFF0000u);
    z0[2] = __uint_as_float(pz[1] << 16); z0[3] = __uint_as_float(pz[1] & 0xFFFF0000u);
    z1[0] = __uint_as_float(pz[2] << 16); z1[1] = __uint_as_float(pz[2] & 0xFFFF0000u);
    z1[2] = __uint_as_float(pz[3] << 16); z1[3] = __uint_as_float(pz[3] & 0xFFFF0000u);
#pragma unroll
    for (int c = 0; c < 4; ++c) { z0[c] += __shfl_xor(z0[c], 4); z1[c] += __shfl_xor(z1[c], 4); }
    char* red = smem + L_K;
    wg_barrier();
    *(f32x4*)(red + w * 2048 + lane * 16) = s0v;
    *(f32x4*)(red + w * 2048 + 1024 + lane * 16) = s1v;
    if (lane < 4) {
      *(f32x4*)(red + 16384 + w * 128 + lane * 32) = z0;
      *(f32x4*)(red + 16384 + w * 128 + lane * 32 + 16) = z1;
    }
    const bf16x8 wf = wphi_frag_lds(smem + L_WPHI2 + (head & 1) * 512, lane);
    f32x16 pq = MFMA32(wf, qa, zero16());
    bf16x8 fp, fn;
    wg_barrier();
    STAMP(7);
    if (w == 0) {
      const char* src = red + lane * 16;
      f32x4 t0 = *(const f32x4*)src, t1 = *(const f32x4*)(src + 1024);
#pragma unroll
      for (int t = 1; t < 8; ++t) { t0 += *(const f32x4*)(src + t * 2048); t1 += *(const f32x4*)(src + t * 2048 + 1024); }
      bf16x8 o;
#pragma unroll
      for (int c = 0; c < 4; ++c) { o[c] = (__bf16)t0[c]; o[4 + c] = (__bf16)t1[c]; }
      *(bf16x8*)(smem + L_KVF + (lane >> 5) * 1024 + (((lane >> 4) & 1) * 32 + (lane & 15)) * 16) = o;
    }
    if (w == 1) {
      const int f = lane & 31;
      const float* zr = (const float*)(red + 16384);
      const __bf16 z = (__bf16)(((zr[f] + zr[32 + f]) + (zr[64 + f] + zr[96 + f])) + ((zr[128 + f] + zr[160 + f]) + (zr[192 + f] + zr[224 + f])));
#pragma unroll
      for (int i = 0; i < 8; ++i)
        *(__bf16*)(smem + L_KVF + (f >> 4) * 1024 + (((f >> 2) & 1) * 32 + 16 + 8 * h + i) * 16 + (((f >> 3) & 1) * 4 + (f & 3)) * 2) = z;
    }
    dual_softmax(pq, fp, fn);
    wg_barrier();
    f32x16 o = MFMA32(lds_read16(smem + L_KVF + lane * 16), fp, zero16());
    o = MFMA32(lds_read16(smem + L_KVF + 1024 + lane * 16), fn, o);
    const float inv = __builtin_amdgcn_rcpf(o[8] + 1e-6f);
    st_sc1_frag(wsr, WS_O + (head * NT + qt) * 1024 + lane * 16, pack8s<0>(o, inv));
    STAMP(8);
    drain_barrier();
    STAMP(9);
    if (w == 0) {
      signal_slots(ws, pslot, pval, lane >= 32);
      wait_slots(ws, cslot, cwant, lane >= 32);
    }
    wg_barrier();
    STAMP(10);
  }

  {
    const int tt = w & 3, kh = w >> 2;
    const int tile32 = ti3 * 4 + tt;
    const uint32_t og = WS_O + br3 * FRAG_BYTES + ((16 * kh) * NT + tile32) * 1024 + lane * 16;
    bf16x8 of[16];
#pragma unroll
    for (int i = 0; i < 16; ++i) of[i] = ld_sc1_frag(wsr, og + i * NT * 1024);
    const int blk = (lane >> 4) & 1, q4 = (lane & 15) >> 2, p4 = lane & 3;
    f32x16 acc0 = zero16(), acc1 = zero16();
    const char* wl = smem + L_WO;
    const int ra0 = (256 * kh + 4 * h + q4) * 128 + (((2 * blk + (p4 >> 1)) ^ (((q4 >> 1) & 1) << 2)) * 16) + 8 * (p4 & 1);
    const int ra1 = (256 * kh + 4 * h + q4) * 128 + (((4 + 2 * blk + (p4 >> 1)) ^ (((q4 >> 1) & 1) << 2)) * 16) + 8 * (p4 & 1);
#pragma unroll
    for (int i = 0; i < 16; ++i) {
      const int kb = 16 * i * 128;
      const bf16x8 a0 = tr_frag(wl + ra0 + kb, wl + ra0 + kb + 1024);
      const bf16x8 a1 = tr_frag(wl + ra1 + kb, wl + ra1 + kb + 1024);
      acc0 = MFMA32(a0, of[i], acc0);
      acc1 = MFMA32(a1, of[i], acc1);
    }
    STAMP(11);
    float* ex = (float*)smem + w * 1024 + lane * 4;
    const f32x16 give = kh ? acc0 : acc1;
    f32x16 keep = kh ? acc1 : acc0;
#pragma unroll
    for (int r4 = 0; r4 < 4; ++r4) {
      f32x4 v = {give[4 * r4], give[4 * r4 + 1], give[4 * r4 + 2], give[4 * r4 + 3]};
      *(f32x4*)(ex + r4 * 256) = v;
    }
    wg_barrier();
    const float* px = (const float*)smem + (w ^ 4) * 1024 + lane * 4;
#pragma unroll
    for (int r4 = 0; r4 < 4; ++r4) {
      const f32x4 v = *(const f32x4*)(px + r4 * 256);
#pragma unroll
      for (int c = 0; c < 4; ++c) keep[4 * r4 + c] += v[c];
    }
    const u32x4 lo = __builtin_bit_cast(u32x4, pack8<0>(keep)), hi = __builtin_bit_cast(u32x4, pack8<8>(keep));
    u16* op = out + (size_t)(tile32 * 32 + (lane & 31)) * 1024 + br3 * 512 + nj * 64 + kh * 32 + h * 8;
    auto t0 = __builtin_amdgcn_permlane32_swap(lo[0], lo[2], false, false);
    auto t1 = __builtin_amdgcn_permlane32_swap(lo[1], lo[3], false, false);
    u32x4 v = {t0[0], t1[0], t0[1], t1[1]};
    *(u32x4*)op = v;
    auto t2 = __builtin_amdgcn_permlane32_swap(hi[0], hi[2], false, false);
    auto t3 = __builtin_amdgcn_permlane32_swap(hi[1], hi[3], false, false);
    u32x4 u = {t2[0], t3[0], t2[1], t3[1]};
    *(u32x4*)(op + 16) = u;
  }
  STAMP(12);
}

extern "C" void kernel_launch(void* const* d_in, const int* in_sizes, int n_in, void* d_out, int out_size,
                              void* d_ws, size_t ws_size, hipStream_t stream) {
  fused<<<256, 512, 0, stream>>>((const u16*)d_in[0], (const u16*)d_in[1], (const u16*)d_in[2], (const u16*)d_in[3],
                                 (const u16*)d_in[4], (const u16*)d_in[5], (const u16*)d_in[6], (char*)d_ws, (u16*)d_out);
}
```

```cpp
#include <hip/hip_runtime.h>
#include <hip/hip_bf16.h>
#include <stdint.h>

typedef __bf16 bf16x8 __attribute__((ext_vector_type(8)));
typedef __bf16 bf16x4 __attribute__((ext_vector_type(4)));
typedef short s16x4 __attribute__((ext_vector_type(4)));
typedef short s16x8 __attribute__((ext_vector_type(8)));
typedef float f32x16 __attribute__((ext_vector_type(16)));
typedef float f32x4 __attribute__((ext_vector_type(4)));
typedef unsigned u32x4 __attribute__((ext_vector_type(4)));
typedef unsigned u32x2 __attribute__((ext_vector_type(2)));
typedef unsigned short u16;

#define LDS_AS __attribute__((address_space(3)))
#define GLB_AS __attribute__((address_space(1)))
#define DEVI __device__ __forceinline__
#define MFMA32(a, b, c) __builtin_amdgcn_mfma_f32_32x32x16_bf16((a), (b), (c), 0, 0, 0)

#define STAMPW(i)
#define STAMP(i)
constexpr int SEQ = 2048, DM = 512, NH = 32, NT = 64;
constexpr float L2E = 1.4426950408889634f;

constexpr uint32_t FRAG_BYTES = NH * NT * 1024;
constexpr uint32_t WS_QA = 0, WS_QB = FRAG_BYTES, WS_KB = 2 * FRAG_BYTES, WS_VB = 3 * FRAG_BYTES;
constexpr uint32_t WS_O = 4 * FRAG_BYTES;
constexpr uint32_t WS_KVP = 6 * FRAG_BYTES;
constexpr uint32_t KVP_BYTES = 1088;
constexpr uint32_t WS_E = WS_KVP + NH * 16 * KVP_BYTES;
constexpr uint32_t WS_F = WS_E + 256 * 64 * 8;
constexpr uint32_t WS_END = WS_F + 256 * 64 * 8;

DEVI void glds16s(const void* sbase, uint32_t voff, uint32_t lds_addr) {
  asm volatile("s_mov_b32 m0, %2\n\ts_nop 0\n\tglobal_load_lds_dwordx4 %0, %1" ::"v"(voff), "s"(sbase), "s"(lds_addr) : "memory");
}
DEVI uint32_t lds_addr_of(const char* p) { return (uint32_t)(uintptr_t)(const LDS_AS char*)p; }
DEVI bf16x8 lds_read16(const char* p) { return *(const bf16x8*)p; }
DEVI bf16x8 tr_frag(const char* p0, const char* p1) {
  s16x4 a = __builtin_amdgcn_ds_read_tr16_b64_v4i16((LDS_AS s16x4*)p0);
  s16x4 b = __builtin_amdgcn_ds_read_tr16_b64_v4i16((LDS_AS s16x4*)p1);
  s16x8 r = __builtin_shufflevector(a, b, 0, 1, 2, 3, 4, 5, 6, 7);
  return __builtin_bit_cast(bf16x8, r);
}
DEVI float swap_max(float x) {
  auto t = __builtin_amdgcn_permlane32_swap(__float_as_uint(x), __float_as_uint(x), false, false);
  return fmaxf(__uint_as_float(t[0]), __uint_as_float(t[1]));
}
DEVI float swap_add(float x) {
  auto t = __builtin_amdgcn_permlane32_swap(__float_as_uint(x), __float_as_uint(x), false, false);
  return __uint_as_float(t[0]) + __uint_as_float(t[1]);
}
template <int O>
DEVI bf16x8 pack8(const f32x16& a) {
  bf16x8 r;
#pragma unroll
  for (int j = 0; j < 8; ++j) r[j] = (__bf16)a[O + j];
  return r;
}
template <int O>
DEVI bf16x8 pack8s(const f32x16& a, float s) {
  bf16x8 r;
#pragma unroll
  for (int j = 0; j < 8; ++j) r[j] = (__bf16)(a[O + j] * s);
  return r;
}
DEVI f32x16 zero16() {
  f32x16 z;
#pragma unroll
  for (int i = 0; i < 16; ++i) z[i] = 0.f;
  return z;
}
DEVI bf16x8 wphi_frag_lds(const char* wl, int lane) {
  const char* p = wl + (4 * (lane >> 5) + ((lane & 15) >> 2)) * 32 + (lane & 3) * 8;
  const u32x4 r = __builtin_bit_cast(u32x4, tr_frag(p, p + 256));
  const unsigned sgn = (lane & 16) ? 0x80008000u : 0u;
  const u32x4 o = {r[0] ^ sgn, r[1] ^ sgn, r[2] ^ sgn, r[3] ^ sgn};
  return __builtin_bit_cast(bf16x8, o);
}
DEVI void dual_softmax(const f32x16& p, bf16x8& fp, bf16x8& fn) {
  float mp = fmaxf(fmaxf(fmaxf(p[0], p[1]), fmaxf(p[2], p[3])), fmaxf(fmaxf(p[4], p[5]), fmaxf(p[6], p[7])));
  float mn = fmaxf(fmaxf(fmaxf(p[8], p[9]), fmaxf(p[10], p[11])), fmaxf(fmaxf(p[12], p[13]), fmaxf(p[14], p[15])));
  mp = swap_max(mp) * L2E;
  mn = swap_max(mn) * L2E;
  f32x16 e;
  float sp = 0.f, sn = 0.f;
#pragma unroll
  for (int j = 0; j < 8; ++j) {
    e[j] = __builtin_amdgcn_exp2f(__builtin_fmaf(p[j], L2E, -mp));
    e[8 + j] = __builtin_amdgcn_exp2f(__builtin_fmaf(p[8 + j], L2E, -mn));
    sp += e[j];
    sn += e[8 + j];
  }
  sp = __builtin_amdgcn_rcpf(swap_add(sp));
  sn = __builtin_amdgcn_rcpf(swap_add(sn));
  fp = pack8s<0>(e, sp);
  fn = pack8s<8>(e, sn);
}

typedef unsigned long long u64;
typedef GLB_AS u64 gu64;
#define RLX_AGENT __ATOMIC_RELAXED, __HIP_MEMORY_SCOPE_AGENT
constexpr int AUX_SC1 = 16;
DEVI u32x4 ld_sc1(__amdgpu_buffer_rsrc_t r, uint32_t off) { return __builtin_amdgcn_raw_buffer_load_b128(r, off, 0, AUX_SC1); }
DEVI bf16x8 ld_sc1_frag(__amdgpu_buffer_rsrc_t r, uint32_t off) { return __builtin_bit_cast(bf16x8, ld_sc1(r, off)); }
DEVI void st_sc1(__amdgpu_buffer_rsrc_t r, uint32_t off, u32x4 v) { __builtin_amdgcn_raw_buffer_store_b128(v, r, off, 0, AUX_SC1); }
DEVI void st_sc1_frag(__amdgpu_buffer_rsrc_t r, uint32_t off, bf16x8 v) { st_sc1(r, off, __builtin_bit_cast(u32x4, v)); }
DEVI void wg_barrier() {
  asm volatile("s_waitcnt lgkmcnt(0)" ::: "memory");
  __builtin_amdgcn_s_barrier();
  asm volatile("" ::: "memory");
}
DEVI void drain_barrier() {
  asm volatile("s_waitcnt vmcnt(0) lgkmcnt(0)" ::: "memory");
  __builtin_amdgcn_s_barrier();
  asm volatile("" ::: "memory");
}
DEVI u64 mix(u64 e) { return e * 0x9E3779B97F4A7C15ull + 0x632BE59BD9B4E019ull; }
DEVI void wait_slots(char* ws, uint32_t slot, u64 want, bool active) {
  const gu64* f = (const gu64*)(ws + WS_F) + slot;
#pragma unroll 1
  for (int it = 0; it < 200000; ++it) {
    const u64 v = active ? __hip_atomic_load(f, RLX_AGENT) : want;
    if (__builtin_amdgcn_ballot_w64(v != want) == 0) break;
    __builtin_amdgcn_s_sleep(1);
  }
  if (active) __hip_atomic_store((gu64*)(ws + WS_E) + slot, want, RLX_AGENT);
}
DEVI void spin_slots(char* ws, uint32_t slot, u64 want, bool active) {
  const gu64* f = (const gu64*)(ws + WS_F) + slot;
#pragma unroll 1
  for (int it = 0; it < 200000; ++it) {
    const u64 v = active ? __hip_atomic_load(f, RLX_AGENT) : want;
    if (__builtin_amdgcn_ballot_w64(v != want) == 0) break;
    __builtin_amdgcn_s_sleep(1);
  }
}
DEVI void signal_slots(char* ws, uint32_t slot, u64 val, bool active) {
  if (active) __hip_atomic_store((gu64*)(ws + WS_F) + slot, val, RLX_AGENT);
}

constexpr int STAGE_BYTES = 40960;
constexpr int L_WPHI = 3 * STAGE_BYTES;
constexpr int L_K = 0, L_V = 16384, L_ONES = 32768, L_RED = 33792, L_ZRED = L_RED + 8192, L_KVF = L_ZRED + 512, L_WO = 57344;
constexpr int L_WPHI2 = L_WPHI + 2048;
constexpr int LDS_BYTES = L_WPHI2 + 1024;

__global__ __launch_bounds__(512, 2) void fused(const u16* __restrict__ xA, const u16* __restrict__ wA,
                                                const u16* __restrict__ wphi, const u16* __restrict__ woA,
                                                const u16* __restrict__ xB, const u16* __restrict__ wB,
                                                const u16* __restrict__ woB, char* __restrict__ ws,
                                                u16* __restrict__ out) {
  __shared__ __attribute__((aligned(16))) char smem[LDS_BYTES];
  const int tid = threadIdx.x, lane = tid & 63;
  const int w = __builtin_amdgcn_readfirstlane(tid >> 6);
  const int bid = blockIdx.x, xcd = bid & 7, idx = bid >> 3;
  const int h = lane >> 5;
  const int br1 = xcd >> 2, jx = xcd & 3;
  const int ti1 = (jx & 1) * 8 + (idx & 7);
  const int g1 = (jx >> 1) * 4 + (idx >> 3);
  const int head = bid & 31, chunk = bid >> 5;
  const int br3 = xcd >> 2, ti3 = (xcd & 3) * 4 + (idx & 3), nj = idx >> 2;
  const __amdgpu_buffer_rsrc_t wsr = __builtin_amdgcn_make_buffer_rsrc(ws, 0, WS_END, 0x00020000);
  const uint32_t sm0 = lds_addr_of(smem);

  STAMP(0);
  const uint32_t cslot = bid * 64 + lane;
  uint32_t pslot;
  bool pact;
  {
    const int cc = (ti1 >> 1) + (lane >> 2);
    const int j = lane - 32, tj = 2 * chunk + ((j >> 3) & 1);
    const int c3 = ((j & 7) * 4 + (tj & 3)) * 8 + (j >> 4) * 4 + (tj >> 2);
    if (lane >= 32) { pact = true; pslot = c3 * 64 + 32 + head; }
    else if (br1) { pact = lane < 8 && cc <= 7; pslot = (cc * 32 + 4 * g1 + (lane & 3)) * 64 + (ti1 - 2 * cc + 2); }
    else { pact = true; pslot = ((lane >> 2) * 32 + 4 * g1 + (lane & 3)) * 64 + 16 + ti1; }
    if (!pact) pslot = cslot;
  }
  u64 cwant = 0, pval = 0;
  cwant = __hip_atomic_load((const gu64*)(ws + WS_E) + cslot, RLX_AGENT);
  if (w == 0) pval = __hip_atomic_load((const gu64*)(ws + WS_E) + pslot, RLX_AGENT);

  {
    const u16* x = br1 ? xB : xA;
    const u16* W = br1 ? wB : wA;
    const int tt = w & 3, hp = w >> 2;
    const uint32_t xo0 = (uint32_t)((ti1 * 128 + 16 * w + (lane >> 3)) * DM + (((lane & 7) ^ ((lane >> 4) & 7)) * 8)) * 2u;
    const uint32_t xo1 = (uint32_t)((ti1 * 128 + 16 * w + 8 + (lane >> 3)) * DM + (((lane & 7) ^ ((4 + (lane >> 4)) & 7)) * 8)) * 2u;
    const uint32_t wo0 = (uint32_t)((8 * w + (lane >> 3)) * 1536 + g1 * 64 + (((lane & 7) ^ (((lane >> 4) & 1) << 2)) * 8)) * 2u;
    const int s0 = ((idx & 7) + 2 * (idx >> 3)) & 7;
    auto stage_load = [&](int su, int buf, int part) {
      const int s = (su + s0) & 7;
      const uint32_t b = sm0 + buf * STAGE_BYTES;
      if (part == 0) {
        glds16s(x + s * 64, xo0, b + w * 2048);
        glds16s(x + s * 64, xo1, b + w * 2048 + 1024);
      } else if (part == 1) {
        glds16s(W + (size_t)s * 64 * 1536, wo0, b + 16384 + w * 1024);
        glds16s(W + (size_t)s * 64 * 1536 + 512, wo0, b + 16384 + 8192 + w * 1024);
      } else {
        glds16s(W + (size_t)s * 64 * 1536 + 1024, wo0, b + 16384 + 16384 + w * 1024);
      }
    };
    if (w == 1) glds16s(wphi + (size_t)(head & ~1) * 256, (uint32_t)lane * 16u, sm0 + L_WPHI2);
    if (br1 == 0 && w >= 6) glds16s(wphi + (size_t)(4 * g1) * 256 + (w - 6) * 512, (uint32_t)lane * 16u, sm0 + L_WPHI + (w - 6) * 1024);
    for (int p = 0; p < 3; ++p) stage_load(0, 0, p);
    for (int p = 0; p < 3; ++p) stage_load(1, 1, p);

    const int xrow = 32 * tt + (lane & 31);
    const int xa0 = xrow * 128 + ((h ^ ((xrow >> 1) & 7)) * 16);
    const int q4 = (lane & 15) >> 2, p4 = lane & 3;
    const int wa0 = 16384 + (8 * h + q4) * 128 + (((4 * hp + 2 * ((lane >> 4) & 1) + (p4 >> 1)) ^ (((q4 >> 1) & 1) << 2)) * 16) + 8 * (p4 & 1);

    f32x16 aq = zero16(), ak = zero16(), av = zero16();
    bf16x8 xf[2], fq[2], fk[2], fv[2];
    auto frags = [&](const char* b, int ks, int p) {
      xf[p] = lds_read16(b + (xa0 ^ (32 * ks)));
      const char* wb = b + wa0 + ks * 2048;
      fq[p] = tr_frag(wb, wb + 512);
      fk[p] = tr_frag(wb + 8192, wb + 8192 + 512);
      fv[p] = tr_frag(wb + 16384, wb + 16384 + 512);
    };
    auto mma = [&](int p) {
      aq = MFMA32(fq[p], xf[p], aq);
      ak = MFMA32(fk[p], xf[p], ak);
      av = MFMA32(xf[p], fv[p], av);
    };
#pragma unroll
    for (int s = 0; s < 8; ++s) {
      if (s < 7) asm volatile("s_waitcnt vmcnt(5)" ::: "memory");
      else asm volatile("s_waitcnt vmcnt(0)" ::: "memory");
      __builtin_amdgcn_s_barrier();
      asm volatile("" ::: "memory");
      if (s == 0) STAMP(1);
      const char* b = smem + (s % 3) * STAGE_BYTES;
      frags(b, 0, 0);
      if (s + 2 < 8) stage_load(s + 2, (s + 2) % 3, 0);
      __builtin_amdgcn_sched_barrier(0);
      if (s > 0) mma(1);
      __builtin_amdgcn_sched_barrier(0);
      frags(b, 1, 1);
      if (s + 2 < 8) stage_load(s + 2, (s + 2) % 3, 1);
      __builtin_amdgcn_sched_barrier(0);
      mma(0);
      __builtin_amdgcn_sched_barrier(0);
      frags(b, 2, 0);
      if (s + 2 < 8) stage_load(s + 2, (s + 2) % 3, 2);
      __builtin_amdgcn_sched_barrier(0);
      mma(1);
      __builtin_amdgcn_sched_barrier(0);
      frags(b, 3, 1);
      __builtin_amdgcn_sched_barrier(0);
      mma(0);
      __builtin_amdgcn_sched_barrier(0);
      asm volatile("s_waitcnt lgkmcnt(0)" ::: "memory");
    }
    mma(1);
    STAMP(2);
    const int tile32 = ti1 * 4 + tt, head0 = 4 * g1 + 2 * hp, blk = (lane >> 4) & 1;
    if (br1) {
      const uint32_t qo = (head0 * NT + tile32) * 1024 + lane * 16;
      st_sc1_frag(wsr, WS_QB + qo, pack8s<0>(aq, 0.25f * L2E));
      st_sc1_frag(wsr, WS_QB + qo + NT * 1024, pack8s<8>(aq, 0.25f * L2E));
      st_sc1_frag(wsr, WS_KB + qo, pack8<0>(ak));
      st_sc1_frag(wsr, WS_KB + qo + NT * 1024, pack8<8>(ak));
      const uint32_t vo = WS_VB + ((head0 + ((lane >> 4) & 1)) * NT + tile32) * 1024 + h * 256 + (lane & 15) * 16;
      st_sc1_frag(wsr, vo, pack8<0>(av));
      st_sc1_frag(wsr, vo + 512, pack8<8>(av));
    } else {
      const uint32_t qo = WS_QA + (head0 * NT + tile32) * 1024 + lane * 16;
      st_sc1_frag(wsr, qo, pack8<0>(aq));
      st_sc1_frag(wsr, qo + NT * 1024, pack8<8>(aq));
      s16x8 i0, i1;
#pragma unroll
      for (int j = 0; j < 8; ++j) {
        const int f = 8 * (j >> 2) + 4 * h + (j & 3);
        i0[j] = ((lane & 31) == f) ? (short)0x3F80 : (short)0;
        i1[j] = ((lane & 31) == 16 + f) ? (short)0x3F80 : (short)0;
      }
      const bf16x8 id0 = __builtin_bit_cast(bf16x8, i0), id1 = __builtin_bit_cast(bf16x8, i1);
      const u32x4 ones = {0x3F803F80u, 0x3F803F80u, 0x3F803F80u, 0x3F803F80u};
      const u32x4 vf0 = __builtin_bit_cast(u32x4, pack8<0>(av)), vf1 = __builtin_bit_cast(u32x4, pack8<8>(av));
      f32x16 pk0 = MFMA32(wphi_frag_lds(smem + L_WPHI + (2 * hp) * 512, lane), pack8<0>(ak), zero16());
      f32x16 pk1 = MFMA32(wphi_frag_lds(smem + L_WPHI + (2 * hp + 1) * 512, lane), pack8<8>(ak), zero16());
      bf16x8 fp[2], fn[2];
      dual_softmax(pk0, fp[0], fn[0]);
      dual_softmax(pk1, fp[1], fn[1]);
#pragma unroll
      for (int p = 0; p < 2; ++p) {
        f32x16 T = MFMA32(fp[p], id0, zero16());
        T = MFMA32(fn[p], id1, T);
        const bool mine = blk == p;
        f32x16 kvp = MFMA32(pack8<0>(T), __builtin_bit_cast(bf16x8, mine ? vf0 : ones), zero16());
        kvp = MFMA32(pack8<8>(T), __builtin_bit_cast(bf16x8, mine ? vf1 : ones), kvp);
        if (mine || (lane & 15) == 0) {
          char* dst = smem + ((2 * hp + p) * 4 + tt) * 2176 + (mine ? (h * 16 + (lane & 15)) * 16 : 2048 + h * 16);
          const int st = mine ? 512 : 32;
#pragma unroll
          for (int r4 = 0; r4 < 4; ++r4) {
            f32x4 v = {kvp[4 * r4], kvp[4 * r4 + 1], kvp[4 * r4 + 2], kvp[4 * r4 + 3]};
            *(f32x4*)(dst + r4 * st) = v;
          }
        }
      }
      wg_barrier();
      if (w < 4 || (w == 4 && lane < 16)) {
        const int hh = w < 4 ? w : (lane >> 2);
        const char* src = smem + hh * 4 * 2176 + (w < 4 ? (lane >> 5) * 1024 + (lane & 31) * 16 : 2048 + (lane & 3) * 32);
        const int d2 = w < 4 ? 512 : 16;
        f32x4 s0v = *(const f32x4*)src, s1v = *(const f32x4*)(src + d2);
#pragma unroll
        for (int t = 1; t < 4; ++t) {
          s0v += *(const f32x4*)(src + t * 2176);
          s1v += *(const f32x4*)(src + t * 2176 + d2);
        }
        bf16x8 o;
#pragma unroll
        for (int c = 0; c < 4; ++c) { o[c] = (__bf16)s0v[c]; o[4 + c] = (__bf16)s1v[c]; }
        st_sc1_frag(wsr, WS_KVP + ((4 * g1 + hh) * 16 + ti1) * KVP_BYTES + (w < 4 ? lane * 16 : 1024 + (lane & 3) * 16), o);
      }
    }
    if (br1) drain_barrier();
    else wg_barrier();
    STAMP(3);
    cwant = mix(cwant);
    if (w == 0) {
      pval = mix(pval);
      if (br1) signal_slots(ws, pslot, pval, pact && lane < 32);
      wait_slots(ws, cslot, cwant, lane < 4 && 2 * chunk - 2 + lane >= 0);
    }
    if (tid >= 448) {
      const u32x4 ones = {0x3F803F80u, 0x3F803F80u, 0x3F803F80u, 0x3F803F80u};
      *(u32x4*)(smem + L_ONES + (tid - 448) * 16) = ones;
    }
    drain_barrier();
    STAMP(4);
    if (br1 == 0 && w == 0) signal_slots(ws, pslot, pval, lane < 32);
  }

  const int qt = chunk * 8 + w;
  const bool lower = (lane & 16) == 0;
  bf16x8 qf;
  {
    const int t0 = chunk * 8 - 8;
    u32x4 kv[4];
    if (t0 + 2 * w >= 0) {
      const uint32_t o = (head * NT + t0 + 2 * w) * 1024 + lane * 16;
      kv[0] = ld_sc1(wsr, WS_KB + o);
      kv[1] = ld_sc1(wsr, WS_KB + o + 1024);
      kv[2] = ld_sc1(wsr, WS_VB + o);
      kv[3] = ld_sc1(wsr, WS_VB + o + 1024);
    }
    qf = ld_sc1_frag(wsr, WS_QB + (head * NT + qt) * 1024 + lane * 16);
    if (t0 + 2 * w >= 0) {
      *(u32x4*)(smem + L_K + 2 * w * 1024 + lane * 16) = kv[0];
      *(u32x4*)(smem + L_K + 2 * w * 1024 + 1024 + lane * 16) = kv[1];
      *(u32x4*)(smem + L_V + 2 * w * 1024 + lane * 16) = kv[2];
      *(u32x4*)(smem + L_V + 2 * w * 1024 + 1024 + lane * 16) = kv[3];
    }
    wg_barrier();
    STAMP(5);
  }
  const bool aslot = lane >= 16 && lane < 32;
  const u64 early = __hip_atomic_load((const gu64*)(ws + WS_F) + cslot, RLX_AGENT);
  u32x4 pp[2], pz;
  bf16x8 qa;
  const u16* wo = br3 ? woB : woA;
  auto wo_load = [&]() {
    const uint32_t off = (uint32_t)((64 * w + (lane >> 3)) * DM + nj * 64 + (((lane & 7) ^ (((lane >> 4) & 1) << 2)) * 8)) * 2u;
#pragma unroll
    for (int i = 0; i < 8; ++i) glds16s(wo + i * 8 * DM, off, sm0 + L_WO + w * 8192 + i * 1024);
  };

  {
    const char* kbase = smem + L_K + w * 1024 + lane * 16;
    const int vinc = lower ? 1024 : 0;
    const char* vbase = lower ? (smem + L_V + w * 1024 + h * 256 + (lane & 15) * 16) : (smem + L_ONES + (lane & 15) * 16);
    const int ql = lane & 31;
    const int first = chunk > 0 ? 0 : 8 - w;
    auto a_side = [&]() {
      if (__builtin_amdgcn_ballot_w64(aslot && early != cwant) != 0) spin_slots(ws, cslot, cwant, aslot);
      asm volatile("" ::: "memory");
      if (w == 0 && aslot) __hip_atomic_store((gu64*)(ws + WS_E) + cslot, cwant, RLX_AGENT);
      const uint32_t kb = WS_KVP + (head * 16 + 2 * w) * KVP_BYTES;
      pp[0] = ld_sc1(wsr, kb + lane * 16);
      pp[1] = ld_sc1(wsr, kb + KVP_BYTES + lane * 16);
      pz = ld_sc1(wsr, kb + ((lane >> 2) & 1) * KVP_BYTES + 1024 + (lane & 3) * 16);
      qa = ld_sc1_frag(wsr, WS_QA + (head * NT + qt) * 1024 + lane * 16);
    };
    auto boundary_p = [&](const f32x16& sm, float m, u32x4& da, u32x4& db, u32x4& ua, u32x4& ub) {
      f32x16 e;
#pragma unroll
      for (int r = 0; r < 16; ++r) e[r] = __builtin_amdgcn_exp2f(sm[r] - m);
      const u32x4 pa = __builtin_bit_cast(u32x4, pack8<0>(e)), pb = __builtin_bit_cast(u32x4, pack8<8>(e));
#pragma unroll
      for (int i = 0; i < 4; ++i) {
        const int ra = 2 * i, rb = 8 + 2 * i;
        const int ka = (ra & 3) + 8 * (ra >> 2) + 4 * h, kb = (rb & 3) + 8 * (rb >> 2) + 4 * h;
        const unsigned ma = (ka <= ql ? 0xFFFFu : 0u) | (ka + 1 <= ql ? 0xFFFF0000u : 0u);
        const unsigned mb = (kb <= ql ? 0xFFFFu : 0u) | (kb + 1 <= ql ? 0xFFFF0000u : 0u);
        da[i] = pa[i] & ma; ua[i] = pa[i] & ~ma;
        db[i] = pb[i] & mb; ub[i] = pb[i] & ~mb;
      }
    };
    f32x16 o = zero16();
    if (chunk > 0) {
      bf16x8 kf[9];
      kf[8] = lds_read16(kbase + 8 * 1024);
      kf[0] = lds_read16(kbase);
      kf[1] = lds_read16(kbase + 1024);
      kf[2] = lds_read16(kbase + 2048);
      f32x16 sm = MFMA32(kf[8], qf, zero16());
      f32x16 s0 = MFMA32(kf[0], qf, zero16());
      f32x16 sc = MFMA32(kf[1], qf, zero16());
#pragma unroll
      for (int r = 0; r < 16; ++r) {
        const int kl = (r & 3) + 8 * (r >> 2) + 4 * h;
        sm[r] = (kl <= ql) ? sm[r] : s0[r];
      }
      float m = sm[0];
#pragma unroll
      for (int r = 1; r < 16; ++r) m = fmaxf(m, sm[r]);
#pragma unroll
      for (int i = 1; i < 8; ++i) {
        f32x16 sn;
        if (i + 2 < 8) kf[i + 2] = lds_read16(kbase + (i + 2) * 1024);
        if (i < 7) sn = MFMA32(kf[i + 1], qf, zero16());
        __builtin_amdgcn_sched_barrier(0);
#pragma unroll
        for (int r = 0; r < 16; r += 2) m = fmaxf(fmaxf(m, sc[r]), sc[r + 1]);
        __builtin_amdgcn_sched_barrier(0);
        if (i < 7) sc = sn;
      }
      m = swap_max(m);
      a_side();
      f32x16 negm;
#pragma unroll
      for (int r = 0; r < 16; ++r) negm[r] = -m;
      sc = MFMA32(kf[1], qf, negm);
      {
        const bf16x8 vd0 = lds_read16(vbase + 8 * vinc), vd1 = lds_read16(vbase + 8 * vinc + 512);
        const bf16x8 vu0 = lds_read16(vbase), vu1 = lds_read16(vbase + 512);
        u32x4 da, db, ua, ub;
        boundary_p(sm, m, da, db, ua, ub);
        o = MFMA32(vd0, __builtin_bit_cast(bf16x8, da), o);
        o = MFMA32(vd1, __builtin_bit_cast(bf16x8, db), o);
        o = MFMA32(vu0, __builtin_bit_cast(bf16x8, ua), o);
        o = MFMA32(vu1, __builtin_bit_cast(bf16x8, ub), o);
      }
#pragma unroll
      for (int i = 1; i < 8; ++i) {
        const bf16x8 v0 = lds_read16(vbase + i * vinc), v1 = lds_read16(vbase + i * vinc + 512);
        f32x16 sn;
        if (i < 7) sn = MFMA32(kf[i + 1], qf, negm);
        __builtin_amdgcn_sched_barrier(0);
        f32x16 e;
#pragma unroll
        for (int r = 0; r < 8; ++r) e[r] = __builtin_amdgcn_exp2f(sc[r]);
        o = MFMA32(v0, pack8<0>(e), o);
#pragma unroll
        for (int r = 8; r < 16; ++r) e[r] = __builtin_amdgcn_exp2f(sc[r]);
        o = MFMA32(v1, pack8<8>(e), o);
        __builtin_amdgcn_sched_barrier(0);
        if (i < 7) sc = sn;
      }
    } else {
      f32x16 sm = MFMA32(lds_read16(kbase + 8 * 1024), qf, zero16());
#pragma unroll
      for (int r = 0; r < 16; ++r) {
        const int kl = (r & 3) + 8 * (r >> 2) + 4 * h;
        sm[r] = (kl <= ql) ? sm[r] : -INFINITY;
      }
      float m = sm[0];
#pragma unroll
      for (int r = 1; r < 16; ++r) m = fmaxf(m, sm[r]);
#pragma unroll 1
      for (int i = first; i < 8; ++i) {
        const f32x16 s = MFMA32(lds_read16(kbase + i * 1024), qf, zero16());
#pragma unroll
        for (int r = 0; r < 16; r += 2) m = fmaxf(fmaxf(m, s[r]), s[r + 1]);
      }
      m = swap_max(m);
      a_side();
      u32x4 da, db, ua, ub;
      boundary_p(sm, m, da, db, ua, ub);
      o = MFMA32(lds_read16(vbase + 8 * vinc), __builtin_bit_cast(bf16x8, da), o);
      o = MFMA32(lds_read16(vbase + 8 * vinc + 512), __builtin_bit_cast(bf16x8, db), o);
#pragma unroll 1
      for (int i = first; i < 8; ++i) {
        const f32x16 s = MFMA32(lds_read16(kbase + i * 1024), qf, zero16());
        f32x16 e;
#pragma unroll
        for (int r = 0; r < 16; ++r) e[r] = __builtin_amdgcn_exp2f(s[r] - m);
        o = MFMA32(lds_read16(vbase + i * vinc), pack8<0>(e), o);
        o = MFMA32(lds_read16(vbase + i * vinc + 512), pack8<8>(e), o);
      }
    }
    const float inv = __builtin_amdgcn_rcpf(o[8]);
    st_sc1_frag(wsr, WS_O + FRAG_BYTES + (head * NT + qt) * 1024 + lane * 16, pack8s<0>(o, inv));
  }
  STAMP(6);
  STAMPW(16);

  {
    f32x4 s0v, s1v, z0, z1;
    s0v[0] = __uint_as_float(pp[0][0] << 16) + __uint_as_float(pp[1][0] << 16);
    s0v[1] = __uint_as_float(pp[0][0] & 0xFFFF0000u) + __uint_as_float(pp[1][0] & 0xFFFF0000u);
    s0v[2] = __uint_as_float(pp[0][1] << 16) + __uint_as_float(pp[1][1] << 16);
    s0v[3] = __uint_as_float(pp[0][1] & 0xFFFF0000u) + __uint_as_float(pp[1][1] & 0xFFFF0000u);
    s1v[0] = __uint_as_float(pp[0][2] << 16) + __uint_as_float(pp[1][2] << 16);
    s1v[1] = __uint_as_float(pp[0][2] & 0xFFFF0000u) + __uint_as_float(pp[1][2] & 0xFFFF0000u);
    s1v[2] = __uint_as_float(pp[0][3] << 16) + __uint_as_float(pp[1][3] << 16);
    s1v[3] = __uint_as_float(pp[0][3] & 0xFFFF0000u) + __uint_as_float(pp[1][3] & 0xFFFF0000u);
    z0[0] = __uint_as_float(pz[0] << 16); z0[1] = __uint_as_float(pz[0] & 0xFFFF0000u);
    z0[2] = __uint_as_float(pz[1] << 16); z0[3] = __uint_as_float(pz[1] & 0xFFFF0000u);
    z1[0] = __uint_as_float(pz[2] << 16); z1[1] = __uint_as_float(pz[2] & 0xFFFF0000u);
    z1[2] = __uint_as_float(pz[3] << 16); z1[3] = __uint_as_float(pz[3] & 0xFFFF0000u);
#pragma unroll
    for (int c = 0; c < 4; ++c) { z0[c] += __shfl_xor(z0[c], 4); z1[c] += __shfl_xor(z1[c], 4); }
    asm volatile("" : "+v"(s0v), "+v"(s1v), "+v"(z0), "+v"(z1));
    wo_load();
    char* red = smem + L_K;
    wg_barrier();
    *(f32x4*)(red + w * 2048 + lane * 16) = s0v;
    *(f32x4*)(red + w * 2048 + 1024 + lane * 16) = s1v;
    if (lane < 4) {
      *(f32x4*)(red + 16384 + w * 128 + lane * 32) = z0;
      *(f32x4*)(red + 16384 + w * 128 + lane * 32 + 16) = z1;
    }
    const bf16x8 wf = wphi_frag_lds(smem + L_WPHI2 + (head & 1) * 512, lane);
    f32x16 pq = MFMA32(wf, qa, zero16());
    bf16x8 fp, fn;
    wg_barrier();
    STAMP(7);
    if (w == 0) {
      const char* src = red + lane * 16;
      f32x4 t0 = *(const f32x4*)src, t1 = *(const f32x4*)(src + 1024);
#pragma unroll
      for (int t = 1; t < 8; ++t) { t0 += *(const f32x4*)(src + t * 2048); t1 += *(const f32x4*)(src + t * 2048 + 1024); }
      bf16x8 o;
#pragma unroll
      for (int c = 0; c < 4; ++c) { o[c] = (__bf16)t0[c]; o[4 + c] = (__bf16)t1[c]; }
      *(bf16x8*)(smem + L_KVF + (lane >> 5) * 1024 + (((lane >> 4) & 1) * 32 + (lane & 15)) * 16) = o;
    }
    if (w == 1) {
      const int f = lane & 31;
      const float* zr = (const float*)(red + 16384);
      const __bf16 z = (__bf16)(((zr[f] + zr[32 + f]) + (zr[64 + f] + zr[96 + f])) + ((zr[128 + f] + zr[160 + f]) + (zr[192 + f] + zr[224 + f])));
#pragma unroll
      for (int i = 0; i < 8; ++i)
        *(__bf16*)(smem + L_KVF + (f >> 4) * 1024 + (((f >> 2) & 1) * 32 + 16 + 8 * h + i) * 16 + (((f >> 3) & 1) * 4 + (f & 3)) * 2) = z;
    }
    dual_softmax(pq, fp, fn);
    wg_barrier();
    f32x16 o = MFMA32(lds_read16(smem + L_KVF + lane * 16), fp, zero16());
    o = MFMA32(lds_read16(smem + L_KVF + 1024 + lane * 16), fn, o);
    const float inv = __builtin_amdgcn_rcpf(o[8] + 1e-6f);
    st_sc1_frag(wsr, WS_O + (head * NT + qt) * 1024 + lane * 16, pack8s<0>(o, inv));
    STAMP(8);
    drain_barrier();
    STAMP(9);
    if (w == 0) {
      signal_slots(ws, pslot, pval, lane >= 32);
      wait_slots(ws, cslot, cwant, lane >= 32);
    }
    wg_barrier();
    STAMP(10);
  }

  {
    const int tt = w & 3, kh = w >> 2;
    const int tile32 = ti3 * 4 + tt;
    const uint32_t og = WS_O + br3 * FRAG_BYTES + ((16 * kh) * NT + tile32) * 1024 + lane * 16;
    bf16x8 of[16];
#pragma unroll
    for (int i = 0; i < 16; ++i) of[i] = ld_sc1_frag(wsr, og + i * NT * 1024);
    __builtin_amdgcn_sched_barrier(0);
    const int blk = (lane >> 4) & 1, q4 = (lane & 15) >> 2, p4 = lane & 3;
    f32x16 acc0 = zero16(), acc1 = zero16();
    const char* wl = smem + L_WO;
    const int ra0 = (256 * kh + 4 * h + q4) * 128 + (((2 * blk + (p4 >> 1)) ^ (((q4 >> 1) & 1) << 2)) * 16) + 8 * (p4 & 1);
    const int ra1 = (256 * kh + 4 * h + q4) * 128 + (((4 + 2 * blk + (p4 >> 1)) ^ (((q4 >> 1) & 1) << 2)) * 16) + 8 * (p4 & 1);
#pragma unroll
    for (int i = 0; i < 16; ++i) {
      const int kb = 16 * i * 128;
      const bf16x8 a0 = tr_frag(wl + ra0 + kb, wl + ra0 + kb + 1024);
      const bf16x8 a1 = tr_frag(wl + ra1 + kb, wl + ra1 + kb + 1024);
      acc0 = MFMA32(a0, of[i], acc0);
      acc1 = MFMA32(a1, of[i], acc1);
    }
    STAMP(11);
    float* ex = (float*)smem + w * 1024 + lane * 4;
    const f32x16 give = kh ? acc0 : acc1;
    f32x16 keep = kh ? acc1 : acc0;
#pragma unroll
    for (int r4 = 0; r4 < 4; ++r4) {
      f32x4 v = {give[4 * r4], give[4 * r4 + 1], give[4 * r4 + 2], give[4 * r4 + 3]};
      *(f32x4*)(ex + r4 * 256) = v;
    }
    wg_barrier();
    const float* px = (const float*)smem + (w ^ 4) * 1024 + lane * 4;
#pragma unroll
    for (int r4 = 0; r4 < 4; ++r4) {
      const f32x4 v = *(const f32x4*)(px + r4 * 256);
#pragma unroll
      for (int c = 0; c < 4; ++c) keep[4 * r4 + c] += v[c];
    }
    const u32x4 lo = __builtin_bit_cast(u32x4, pack8<0>(keep)), hi = __builtin_bit_cast(u32x4, pack8<8>(keep));
    u16* op = out + (size_t)(tile32 * 32 + (lane & 31)) * 1024 + br3 * 512 + nj * 64 + kh * 32 + h * 8;
    auto t0 = __builtin_amdgcn_permlane32_swap(lo[0], lo[2], false, false);
    auto t1 = __builtin_amdgcn_permlane32_swap(lo[1], lo[3], false, false);
    u32x4 v = {t0[0], t1[0], t0[1], t1[1]};
    *(u32x4*)op = v;
    auto t2 = __builtin_amdgcn_permlane32_swap(hi[0], hi[2], false, false);
    auto t3 = __builtin_amdgcn_permlane32_swap(hi[1], hi[3], false, false);
    u32x4 u = {t2[0], t3[0], t2[1], t3[1]};
    *(u32x4*)(op + 16) = u;
  }
  STAMP(12);
}

extern "C" void kernel_launch(void* const* d_in, const int* in_sizes, int n_in, void* d_out, int out_size,
                              void* d_ws, size_t ws_size, hipStream_t stream) {
  fused<<<256, 512, 0, stream>>>((const u16*)d_in[0], (const u16*)d_in[1], (const u16*)d_in[2], (const u16*)d_in[3],
                                 (const u16*)d_in[4], (const u16*)d_in[5], (const u16*)d_in[6], (char*)d_ws, (u16*)d_out);
}
```

```cpp
#include <hip/hip_runtime.h>
#include <hip/hip_bf16.h>
#include <stdint.h>

typedef __bf16 bf16x8 __attribute__((ext_vector_type(8)));
typedef __bf16 bf16x4 __attribute__((ext_vector_type(4)));
typedef short s16x4 __attribute__((ext_vector_type(4)));
typedef short s16x8 __attribute__((ext_vector_type(8)));
typedef float f32x16 __attribute__((ext_vector_type(16)));
typedef float f32x4 __attribute__((ext_vector_type(4)));
typedef unsigned u32x4 __attribute__((ext_vector_type(4)));
typedef unsigned u32x2 __attribute__((ext_vector_type(2)));
typedef unsigned short u16;

#define LDS_AS __attribute__((address_space(3)))
#define GLB_AS __attribute__((address_space(1)))
#define DEVI __device__ __forceinline__
#define MFMA32(a, b, c) __builtin_amdgcn_mfma_f32_32x32x16_bf16((a), (b), (c), 0, 0, 0)

#define STAMPW(i)
#define STAMP(i)
constexpr int SEQ = 2048, DM = 512, NH = 32, NT = 64;
constexpr float L2E = 1.4426950408889634f;

constexpr uint32_t FRAG_BYTES = NH * NT * 1024;
constexpr uint32_t WS_QA = 0, WS_QB = FRAG_BYTES, WS_KB = 2 * FRAG_BYTES, WS_VB = 3 * FRAG_BYTES;
constexpr uint32_t WS_O = 4 * FRAG_BYTES;
constexpr uint32_t WS_KVP = 6 * FRAG_BYTES;
constexpr uint32_t KVP_BYTES = 1088;
constexpr uint32_t WS_E = WS_KVP + NH * 16 * KVP_BYTES;
constexpr uint32_t WS_F = WS_E + 256 * 64 * 8;
constexpr uint32_t WS_END = WS_F + 256 * 64 * 8;

DEVI void glds16s(const void* sbase, uint32_t voff, uint32_t lds_addr) {
  asm volatile("s_mov_b32 m0, %2\n\ts_nop 0\n\tglobal_load_lds_dwordx4 %0, %1" ::"v"(voff), "s"(sbase), "s"(lds_addr) : "memory");
}
DEVI void glds4s(const void* sbase, uint32_t voff, uint32_t lds_addr) {
  asm volatile("s_mov_b32 m0, %2\n\ts_nop 0\n\tglobal_load_lds_dword %0, %1" ::"v"(voff), "s"(sbase), "s"(lds_addr) : "memory");
}
DEVI uint32_t lds_addr_of(const char* p) { return (uint32_t)(uintptr_t)(const LDS_AS char*)p; }
DEVI bf16x8 lds_read16(const char* p) { return *(const bf16x8*)p; }
DEVI bf16x8 tr_frag(const char* p0, const char* p1) {
  s16x4 a = __builtin_amdgcn_ds_read_tr16_b64_v4i16((LDS_AS s16x4*)p0);
  s16x4 b = __builtin_amdgcn_ds_read_tr16_b64_v4i16((LDS_AS s16x4*)p1);
  s16x8 r = __builtin_shufflevector(a, b, 0, 1, 2, 3, 4, 5, 6, 7);
  return __builtin_bit_cast(bf16x8, r);
}
DEVI float max2(float a, float b) { return __builtin_elementwise_maximum(a, b); }
DEVI float max3(float a, float b, float c) { return max2(max2(a, b), c); }
DEVI float swap_max(float x) {
  auto t = __builtin_amdgcn_permlane32_swap(__float_as_uint(x), __float_as_uint(x), false, false);
  return max2(__uint_as_float(t[0]), __uint_as_float(t[1]));
}
DEVI float swap_add(float x) {
  auto t = __builtin_amdgcn_permlane32_swap(__float_as_uint(x), __float_as_uint(x), false, false);
  return __uint_as_float(t[0]) + __uint_as_float(t[1]);
}
typedef float f32x2 __attribute__((ext_vector_type(2)));
typedef __bf16 bf16x2 __attribute__((ext_vector_type(2)));
DEVI unsigned cvt2(float a, float b) {
  const f32x2 v = {a, b};
  return __builtin_bit_cast(unsigned, __builtin_convertvector(v, bf16x2));
}
template <int O>
DEVI bf16x8 pack8(const f32x16& a) {
  const u32x4 r = {cvt2(a[O], a[O + 1]), cvt2(a[O + 2], a[O + 3]), cvt2(a[O + 4], a[O + 5]), cvt2(a[O + 6], a[O + 7])};
  return __builtin_bit_cast(bf16x8, r);
}
template <int O>
DEVI bf16x8 pack8s(const f32x16& a, float s) {
  const u32x4 r = {cvt2(a[O] * s, a[O + 1] * s), cvt2(a[O + 2] * s, a[O + 3] * s), cvt2(a[O + 4] * s, a[O + 5] * s),
                   cvt2(a[O + 6] * s, a[O + 7] * s)};
  return __builtin_bit_cast(bf16x8, r);
}
DEVI f32x16 zero16() {
  f32x16 z;
#pragma unroll
  for (int i = 0; i < 16; ++i) z[i] = 0.f;
  return z;
}
DEVI bf16x8 wphi_frag_lds(const char* wl, int lane) {
  const char* p = wl + (4 * (lane >> 5) + ((lane & 15) >> 2)) * 32 + (lane & 3) * 8;
  const u32x4 r = __builtin_bit_cast(u32x4, tr_frag(p, p + 256));
  const unsigned sgn = (lane & 16) ? 0x80008000u : 0u;
  const u32x4 o = {r[0] ^ sgn, r[1] ^ sgn, r[2] ^ sgn, r[3] ^ sgn};
  return __builtin_bit_cast(bf16x8, o);
}
DEVI void dual_softmax(const f32x16& p, bf16x8& fp, bf16x8& fn) {
  float mp = max3(max3(p[0], p[1], p[2]), max3(p[3], p[4], p[5]), max2(p[6], p[7]));
  float mn = max3(max3(p[8], p[9], p[10]), max3(p[11], p[12], p[13]), max2(p[14], p[15]));
  mp = swap_max(mp) * L2E;
  mn = swap_max(mn) * L2E;
  f32x16 e;
  float sp = 0.f, sn = 0.f;
#pragma unroll
  for (int j = 0; j < 8; ++j) {
    e[j] = __builtin_amdgcn_exp2f(__builtin_fmaf(p[j], L2E, -mp));
    e[8 + j] = __builtin_amdgcn_exp2f(__builtin_fmaf(p[8 + j], L2E, -mn));
    sp += e[j];
    sn += e[8 + j];
  }
  sp = __builtin_amdgcn_rcpf(swap_add(sp));
  sn = __builtin_amdgcn_rcpf(swap_add(sn));
  fp = pack8s<0>(e, sp);
  fn = pack8s<8>(e, sn);
}

typedef unsigned long long u64;
typedef GLB_AS u64 gu64;
#define RLX_AGENT __ATOMIC_RELAXED, __HIP_MEMORY_SCOPE_AGENT
constexpr int AUX_SC1 = 16;
DEVI u32x4 ld_sc1(__amdgpu_buffer_rsrc_t r, uint32_t off) { return __builtin_amdgcn_raw_buffer_load_b128(r, off, 0, AUX_SC1); }
DEVI bf16x8 ld_sc1_frag(__amdgpu_buffer_rsrc_t r, uint32_t off) { return __builtin_bit_cast(bf16x8, ld_sc1(r, off)); }
DEVI void st_sc1(__amdgpu_buffer_rsrc_t r, uint32_t off, u32x4 v) { __builtin_amdgcn_raw_buffer_store_b128(v, r, off, 0, AUX_SC1); }
DEVI void st_sc1_frag(__amdgpu_buffer_rsrc_t r, uint32_t off, bf16x8 v) { st_sc1(r, off, __builtin_bit_cast(u32x4, v)); }
DEVI void wg_barrier() {
  asm volatile("s_waitcnt lgkmcnt(0)" ::: "memory");
  __builtin_amdgcn_s_barrier();
  asm volatile("" ::: "memory");
}
DEVI void drain_barrier() {
  asm volatile("s_waitcnt vmcnt(0) lgkmcnt(0)" ::: "memory");
  __builtin_amdgcn_s_barrier();
  asm volatile("" ::: "memory");
}
DEVI u64 mix(u64 e) { return e * 0x9E3779B97F4A7C15ull + 0x632BE59BD9B4E019ull; }
DEVI void wait_slots(char* ws, uint32_t slot, u64 want, bool active) {
  const gu64* f = (const gu64*)(ws + WS_F) + slot;
#pragma unroll 1
  for (int it = 0; it < 200000; ++it) {
    const u64 v = active ? __hip_atomic_load(f, RLX_AGENT) : want;
    if (__builtin_amdgcn_ballot_w64(v != want) == 0) break;
    __builtin_amdgcn_s_sleep(1);
  }
  if (active) __hip_atomic_store((gu64*)(ws + WS_E) + slot, want, RLX_AGENT);
}
DEVI void spin_slots(char* ws, uint32_t slot, u64 want, bool active) {
  const gu64* f = (const gu64*)(ws + WS_F) + slot;
#pragma unroll 1
  for (int it = 0; it < 200000; ++it) {
    const u64 v = active ? __hip_atomic_load(f, RLX_AGENT) : want;
    if (__builtin_amdgcn_ballot_w64(v != want) == 0) break;
    __builtin_amdgcn_s_sleep(1);
  }
}
DEVI void signal_slots(char* ws, uint32_t slot, u64 val, bool active) {
  if (active) __hip_atomic_store((gu64*)(ws + WS_F) + slot, val, RLX_AGENT);
}

#define PF 1
constexpr uint32_t CODE_PF = 19456;
constexpr int STAGE_BYTES = 40960;
constexpr int L_WPHI = 3 * STAGE_BYTES;
constexpr int L_K = 0, L_V = 16384, L_ONES = 32768, L_RED = 33792, L_KVF = L_RED + 17408, L_WO = 57344;
#define A_AT 3
constexpr int L_WPHI2 = L_WPHI + 2048;
constexpr int L_PF = L_WPHI2 + 1024;
constexpr int LDS_BYTES = L_PF + 256;

__global__ __launch_bounds__(512, 2) void fused(const u16* __restrict__ xA, const u16* __restrict__ wA,
                                                const u16* __restrict__ wphi, const u16* __restrict__ woA,
                                                const u16* __restrict__ xB, const u16* __restrict__ wB,
                                                const u16* __restrict__ woB, char* __restrict__ ws,
                                                u16* __restrict__ out) {
  __shared__ __attribute__((aligned(16))) char smem[LDS_BYTES];
  const int tid = threadIdx.x, lane = tid & 63;
  const int w = __builtin_amdgcn_readfirstlane(tid >> 6);
  const int bid = blockIdx.x, xcd = bid & 7, idx = bid >> 3;
  const int h = lane >> 5;
  const int br1 = xcd >> 2, jx = xcd & 3;
  const int ti1 = (jx & 1) * 8 + (idx & 7);
  const int g1 = (jx >> 1) * 4 + (idx >> 3);
  const int head = bid & 31, chunk = bid >> 5;
  const int br3 = xcd >> 2, ti3 = (xcd & 3) * 4 + (idx & 3), nj = idx >> 2;
  const __amdgpu_buffer_rsrc_t wsr = __builtin_amdgcn_make_buffer_rsrc(ws, 0, WS_END, 0x00020000);
  const uint32_t sm0 = lds_addr_of(smem);

  STAMP(0);
  const uint32_t cslot = bid * 64 + lane;
  uint32_t pslot;
  bool pact;
  {
    const int cc = (ti1 >> 1) + (lane >> 2);
    const int j = lane - 32, tj = 2 * chunk + ((j >> 3) & 1);
    const int c3 = ((j & 7) * 4 + (tj & 3)) * 8 + (j >> 4) * 4 + (tj >> 2);
    if (lane >= 32) { pact = true; pslot = c3 * 64 + 32 + head; }
    else if (br1) { pact = lane < 8 && cc <= 7; pslot = (cc * 32 + 4 * g1 + (lane & 3)) * 64 + (ti1 - 2 * cc + 2); }
    else { pact = true; pslot = ((lane >> 2) * 32 + 4 * g1 + (lane & 3)) * 64 + 16 + ti1; }
    if (!pact) pslot = cslot;
  }
  u64 cwant = 0, pval = 0;

  {
    const u16* x = br1 ? xB : xA;
    const u16* W = br1 ? wB : wA;
    const int tt = w & 3, hp = w >> 2;
    const uint32_t xo0 = (uint32_t)((ti1 * 128 + 16 * w + (lane >> 3)) * DM + (((lane & 7) ^ ((lane >> 4) & 7)) * 8)) * 2u;
    const uint32_t xo1 = (uint32_t)((ti1 * 128 + 16 * w + 8 + (lane >> 3)) * DM + (((lane & 7) ^ ((4 + (lane >> 4)) & 7)) * 8)) * 2u;
    const uint32_t wo0 = (uint32_t)((8 * w + (lane >> 3)) * 1536 + g1 * 64 + (((lane & 7) ^ (((lane >> 4) & 1) << 2)) * 8)) * 2u;
    const int s0 = ((idx & 7) + 2 * (idx >> 3)) & 7;
    auto stage_load = [&](int su, int buf, int part) {
      const int s = (su + s0) & 7;
      const uint32_t b = sm0 + buf * STAGE_BYTES;
      if (part == 0) {
        glds16s(x + s * 64, xo0, b + w * 2048);
        glds16s(x + s * 64, xo1, b + w * 2048 + 1024);
      } else if (part == 1) {
        glds16s(W + (size_t)s * 64 * 1536, wo0, b + 16384 + w * 1024);
        glds16s(W + (size_t)s * 64 * 1536 + 512, wo0, b + 16384 + 8192 + w * 1024);
      } else {
        glds16s(W + (size_t)s * 64 * 1536 + 1024, wo0, b + 16384 + 16384 + w * 1024);
      }
    };
    if (w == 1) glds16s(wphi + (size_t)(head & ~1) * 256, (uint32_t)lane * 16u, sm0 + L_WPHI2);
    if (br1 == 0 && w >= 6) glds16s(wphi + (size_t)(4 * g1) * 256 + (w - 6) * 512, (uint32_t)lane * 16u, sm0 + L_WPHI + (w - 6) * 1024);
    for (int p = 0; p < 3; ++p) stage_load(0, 0, p);
    for (int p = 0; p < 3; ++p) stage_load(1, 1, p);
    {
      const char* pc;
      asm volatile("s_getpc_b64 %0" : "=s"(pc));
      const uint32_t o = (uint32_t)tid * 64u;
      glds4s(pc, o < CODE_PF ? o : 0u, sm0 + L_PF);
    }
    cwant = __hip_atomic_load((const gu64*)(ws + WS_E) + cslot, RLX_AGENT);
    pval = __hip_atomic_load((const gu64*)(ws + WS_E) + pslot, RLX_AGENT);

    const int xrow = 32 * tt + (lane & 31);
    const int xa0 = xrow * 128 + ((h ^ ((xrow >> 1) & 7)) * 16);
    const int q4 = (lane & 15) >> 2, p4 = lane & 3;
    const int wa0 = 16384 + (8 * h + q4) * 128 + (((4 * hp + 2 * ((lane >> 4) & 1) + (p4 >> 1)) ^ (((q4 >> 1) & 1) << 2)) * 16) + 8 * (p4 & 1);

    f32x16 aq = zero16(), ak = zero16(), av = zero16();
    bf16x8 xf[2], fq[2], fk[2], fv[2];
    auto frags = [&](const char* b, int ks, int p) {
      xf[p] = lds_read16(b + (xa0 ^ (32 * ks)));
      const char* wb = b + wa0 + ks * 2048;
      fq[p] = tr_frag(wb, wb + 512);
      fk[p] = tr_frag(wb + 8192, wb + 8192 + 512);
      fv[p] = tr_frag(wb + 16384, wb + 16384 + 512);
    };
    auto mma = [&](int p) {
      aq = MFMA32(fq[p], xf[p], aq);
      ak = MFMA32(fk[p], xf[p], ak);
      av = MFMA32(xf[p], fv[p], av);
    };
#pragma unroll
    for (int s = 0; s < 8; ++s) {
      if (s == 0) asm volatile("s_waitcnt vmcnt(%0)" ::"n"(7 + (PF & 1) + (PF >> 1)) : "memory");
      else if (s < 7) asm volatile("s_waitcnt vmcnt(5)" ::: "memory");
      else asm volatile("s_waitcnt vmcnt(0)" ::: "memory");
      __builtin_amdgcn_s_barrier();
      asm volatile("" ::: "memory");
      if (s == 0) STAMP(1);
      const char* b = smem + (s % 3) * STAGE_BYTES;
      frags(b, 0, 0);
      if (s + 2 < 8) stage_load(s + 2, (s + 2) % 3, 0);
      __builtin_amdgcn_sched_barrier(0);
      if (s > 0) mma(1);
      __builtin_amdgcn_sched_barrier(0);
      frags(b, 1, 1);
      if (s + 2 < 8) stage_load(s + 2, (s + 2) % 3, 1);
      __builtin_amdgcn_sched_barrier(0);
      mma(0);
      __builtin_amdgcn_sched_barrier(0);
      frags(b, 2, 0);
      if (s + 2 < 8) stage_load(s + 2, (s + 2) % 3, 2);
      __builtin_amdgcn_sched_barrier(0);
      mma(1);
      __builtin_amdgcn_sched_barrier(0);
      frags(b, 3, 1);
      __builtin_amdgcn_sched_barrier(0);
      mma(0);
      __builtin_amdgcn_sched_barrier(0);
      asm volatile("s_waitcnt lgkmcnt(0)" ::: "memory");
    }
    mma(1);
    STAMP(2);
    const int tile32 = ti1 * 4 + tt, head0 = 4 * g1 + 2 * hp, blk = (lane >> 4) & 1;
    if (br1) {
      const uint32_t qo = (head0 * NT + tile32) * 1024 + lane * 16;
      st_sc1_frag(wsr, WS_QB + qo, pack8s<0>(aq, 0.25f * L2E));
      st_sc1_frag(wsr, WS_QB + qo + NT * 1024, pack8s<8>(aq, 0.25f * L2E));
      st_sc1_frag(wsr, WS_KB + qo, pack8<0>(ak));
      st_sc1_frag(wsr, WS_KB + qo + NT * 1024, pack8<8>(ak));
      const uint32_t vo = WS_VB + ((head0 + ((lane >> 4) & 1)) * NT + tile32) * 1024 + h * 256 + (lane & 15) * 16;
      st_sc1_frag(wsr, vo, pack8<0>(av));
      st_sc1_frag(wsr, vo + 512, pack8<8>(av));
    } else {
      const uint32_t qo = WS_QA + (head0 * NT + tile32) * 1024 + lane * 16;
      st_sc1_frag(wsr, qo, pack8<0>(aq));
      st_sc1_frag(wsr, qo + NT * 1024, pack8<8>(aq));
      s16x8 i0, i1;
#pragma unroll
      for (int j = 0; j < 8; ++j) {
        const int f = 8 * (j >> 2) + 4 * h + (j & 3);
        i0[j] = ((lane & 31) == f) ? (short)0x3F80 : (short)0;
        i1[j] = ((lane & 31) == 16 + f) ? (short)0x3F80 : (short)0;
      }
      const bf16x8 id0 = __builtin_bit_cast(bf16x8, i0), id1 = __builtin_bit_cast(bf16x8, i1);
      const u32x4 ones = {0x3F803F80u, 0x3F803F80u, 0x3F803F80u, 0x3F803F80u};
      const u32x4 vf0 = __builtin_bit_cast(u32x4, pack8<0>(av)), vf1 = __builtin_bit_cast(u32x4, pack8<8>(av));
      f32x16 pk0 = MFMA32(wphi_frag_lds(smem + L_WPHI + (2 * hp) * 512, lane), pack8<0>(ak), zero16());
      f32x16 pk1 = MFMA32(wphi_frag_lds(smem + L_WPHI + (2 * hp + 1) * 512, lane), pack8<8>(ak), zero16());
      STAMP(13);
      bf16x8 fp[2], fn[2];
      dual_softmax(pk0, fp[0], fn[0]);
      dual_softmax(pk1, fp[1], fn[1]);
      STAMP(14);
#pragma unroll
      for (int p = 0; p < 2; ++p) {
        f32x16 T = MFMA32(fp[p], id0, zero16());
        T = MFMA32(fn[p], id1, T);
        const bool mine = blk == p;
        f32x16 kvp = MFMA32(pack8<0>(T), __builtin_bit_cast(bf16x8, mine ? vf0 : ones), zero16());
        kvp = MFMA32(pack8<8>(T), __builtin_bit_cast(bf16x8, mine ? vf1 : ones), kvp);
        if (mine || (lane & 15) == 0) {
          char* dst = smem + ((2 * hp + p) * 4 + tt) * 2176 + (mine ? (h * 16 + (lane & 15)) * 16 : 2048 + h * 16);
          const int st = mine ? 512 : 32;
#pragma unroll
          for (int r4 = 0; r4 < 4; ++r4) {
            f32x4 v = {kvp[4 * r4], kvp[4 * r4 + 1], kvp[4 * r4 + 2], kvp[4 * r4 + 3]};
            *(f32x4*)(dst + r4 * st) = v;
          }
        }
      }
      wg_barrier();
      STAMP(15);
      if (w < 4 || (w == 4 && lane < 16)) {
        const int hh = w < 4 ? w : (lane >> 2);
        const char* src = smem + hh * 4 * 2176 + (w < 4 ? (lane >> 5) * 1024 + (lane & 31) * 16 : 2048 + (lane & 3) * 32);
        const int d2 = w < 4 ? 512 : 16;
        f32x4 s0v = *(const f32x4*)src, s1v = *(const f32x4*)(src + d2);
#pragma unroll
        for (int t = 1; t < 4; ++t) {
          s0v += *(const f32x4*)(src + t * 2176);
          s1v += *(const f32x4*)(src + t * 2176 + d2);
        }
        bf16x8 o;
#pragma unroll
        for (int c = 0; c < 4; ++c) { o[c] = (__bf16)s0v[c]; o[4 + c] = (__bf16)s1v[c]; }
        st_sc1_frag(wsr, WS_KVP + ((4 * g1 + hh) * 16 + ti1) * KVP_BYTES + (w < 4 ? lane * 16 : 1024 + (lane & 3) * 16), o);
      }
    }
    drain_barrier();
    STAMP(3);
    cwant = mix(cwant);
    if (w == 0) {
      pval = mix(pval);
      signal_slots(ws, pslot, pval, pact && lane < 32);
      wait_slots(ws, cslot, cwant, lane < 4 && 2 * chunk - 2 + lane >= 0);
    }
    if (tid >= 448) {
      const u32x4 ones = {0x3F803F80u, 0x3F803F80u, 0x3F803F80u, 0x3F803F80u};
      *(u32x4*)(smem + L_ONES + (tid - 448) * 16) = ones;
    }
    wg_barrier();
    STAMP(4);
  }

  const int qt = chunk * 8 + w;
  const bool lower = (lane & 16) == 0;
  bf16x8 qf;
  {
    const int t0 = chunk * 8 - 8;
    u32x4 kv[4];
    if (t0 + 2 * w >= 0) {
      const uint32_t o = (head * NT + t0 + 2 * w) * 1024 + lane * 16;
      kv[0] = ld_sc1(wsr, WS_KB + o);
      kv[1] = ld_sc1(wsr, WS_KB + o + 1024);
      kv[2] = ld_sc1(wsr, WS_VB + o);
      kv[3] = ld_sc1(wsr, WS_VB + o + 1024);
    }
    qf = ld_sc1_frag(wsr, WS_QB + (head * NT + qt) * 1024 + lane * 16);
    if (t0 + 2 * w >= 0) {
      *(u32x4*)(smem + L_K + 2 * w * 1024 + lane * 16) = kv[0];
      *(u32x4*)(smem + L_K + 2 * w * 1024 + 1024 + lane * 16) = kv[1];
      *(u32x4*)(smem + L_V + 2 * w * 1024 + lane * 16) = kv[2];
      *(u32x4*)(smem + L_V + 2 * w * 1024 + 1024 + lane * 16) = kv[3];
    }
    wg_barrier();
    STAMP(5);
  }
  const bool aslot = lane >= 16 && lane < 32;
  const u64 early = __hip_atomic_load((const gu64*)(ws + WS_F) + cslot, RLX_AGENT);
  u32x4 pp[2], pz;
  bf16x8 qa;
  const u16* wo = br3 ? woB : woA;
  auto wo_load = [&]() {
    const uint32_t off = (uint32_t)((64 * w + (lane >> 3)) * DM + nj * 64 + (((lane & 7) ^ (((lane >> 4) & 1) << 2)) * 8)) * 2u;
#pragma unroll
    for (int i = 0; i < 8; ++i) glds16s(wo + i * 8 * DM, off, sm0 + L_WO + w * 8192 + i * 1024);
  };

  {
    const char* kbase = smem + L_K + w * 1024 + lane * 16;
    const int vinc = lower ? 1024 : 0;
    const char* vbase = lower ? (smem + L_V + w * 1024 + h * 256 + (lane & 15) * 16) : (smem + L_ONES + (lane & 15) * 16);
    const int ql = lane & 31;
    const int first = chunk > 0 ? 0 : 8 - w;
    auto a_fetch = [&]() {
      if (__builtin_amdgcn_ballot_w64(aslot && early != cwant) != 0) spin_slots(ws, cslot, cwant, aslot);
      asm volatile("" ::: "memory");
      if (w == 0 && aslot) __hip_atomic_store((gu64*)(ws + WS_E) + cslot, cwant, RLX_AGENT);
      const uint32_t kb = WS_KVP + (head * 16 + 2 * w) * KVP_BYTES;
      pp[0] = ld_sc1(wsr, kb + lane * 16);
      pp[1] = ld_sc1(wsr, kb + KVP_BYTES + lane * 16);
      pz = ld_sc1(wsr, kb + ((lane >> 2) & 1) * KVP_BYTES + 1024 + (lane & 3) * 16);
      qa = ld_sc1_frag(wsr, WS_QA + (head * NT + qt) * 1024 + lane * 16);
    };
    auto a_readout = [&]() {
      f32x4 s0v, s1v, z0, z1;
      s0v[0] = __uint_as_float(pp[0][0] << 16) + __uint_as_float(pp[1][0] << 16);
      s0v[1] = __uint_as_float(pp[0][0] & 0xFFFF0000u) + __uint_as_float(pp[1][0] & 0xFFFF0000u);
      s0v[2] = __uint_as_float(pp[0][1] << 16) + __uint_as_float(pp[1][1] << 16);
      s0v[3] = __uint_as_float(pp[0][1] & 0xFFFF0000u) + __uint_as_float(pp[1][1] & 0xFFFF0000u);
      s1v[0] = __uint_as_float(pp[0][2] << 16) + __uint_as_float(pp[1][2] << 16);
      s1v[1] = __uint_as_float(pp[0][2] & 0xFFFF0000u) + __uint_as_float(pp[1][2] & 0xFFFF0000u);
      s1v[2] = __uint_as_float(pp[0][3] << 16) + __uint_as_float(pp[1][3] << 16);
      s1v[3] = __uint_as_float(pp[0][3] & 0xFFFF0000u) + __uint_as_float(pp[1][3] & 0xFFFF0000u);
      z0[0] = __uint_as_float(pz[0] << 16); z0[1] = __uint_as_float(pz[0] & 0xFFFF0000u);
      z0[2] = __uint_as_float(pz[1] << 16); z0[3] = __uint_as_float(pz[1] & 0xFFFF0000u);
      z1[0] = __uint_as_float(pz[2] << 16); z1[1] = __uint_as_float(pz[2] & 0xFFFF0000u);
      z1[2] = __uint_as_float(pz[3] << 16); z1[3] = __uint_as_float(pz[3] & 0xFFFF0000u);
#pragma unroll
      for (int c = 0; c < 4; ++c) { z0[c] += __shfl_xor(z0[c], 4); z1[c] += __shfl_xor(z1[c], 4); }
      char* red = smem + L_RED;
      *(f32x4*)(red + w * 2048 + lane * 16) = s0v;
      *(f32x4*)(red + w * 2048 + 1024 + lane * 16) = s1v;
      if (lane < 4) {
        *(f32x4*)(red + 16384 + w * 128 + lane * 32) = z0;
        *(f32x4*)(red + 16384 + w * 128 + lane * 32 + 16) = z1;
      }
      wo_load();
      const bf16x8 wf = wphi_frag_lds(smem + L_WPHI2 + (head & 1) * 512, lane);
      const f32x16 pq = MFMA32(wf, qa, zero16());
      wg_barrier();
      STAMP(7);
      if (w == 0) {
        const char* src = red + lane * 16;
        f32x4 t0 = *(const f32x4*)src, t1 = *(const f32x4*)(src + 1024);
#pragma unroll
        for (int t = 1; t < 8; ++t) { t0 += *(const f32x4*)(src + t * 2048); t1 += *(const f32x4*)(src + t * 2048 + 1024); }
        bf16x8 o;
#pragma unroll
        for (int c = 0; c < 4; ++c) { o[c] = (__bf16)t0[c]; o[4 + c] = (__bf16)t1[c]; }
        *(bf16x8*)(smem + L_KVF + (lane >> 5) * 1024 + (((lane >> 4) & 1) * 32 + (lane & 15)) * 16) = o;
      }
      if (w == 1) {
        const int f = lane & 31;
        const float* zr = (const float*)(red + 16384);
        const __bf16 z = (__bf16)(((zr[f] + zr[32 + f]) + (zr[64 + f] + zr[96 + f])) + ((zr[128 + f] + zr[160 + f]) + (zr[192 + f] + zr[224 + f])));
#pragma unroll
        for (int i = 0; i < 8; ++i)
          *(__bf16*)(smem + L_KVF + (f >> 4) * 1024 + (((f >> 2) & 1) * 32 + 16 + 8 * h + i) * 16 + (((f >> 3) & 1) * 4 + (f & 3)) * 2) = z;
      }
      bf16x8 fp, fn;
      dual_softmax(pq, fp, fn);
      wg_barrier();
      f32x16 oa = MFMA32(lds_read16(smem + L_KVF + lane * 16), fp, zero16());
      oa = MFMA32(lds_read16(smem + L_KVF + 1024 + lane * 16), fn, oa);
      const float inv = __builtin_amdgcn_rcpf(oa[8] + 1e-6f);
      st_sc1_frag(wsr, WS_O + (head * NT + qt) * 1024 + lane * 16, pack8s<0>(oa, inv));
      STAMP(8);
    };
    auto boundary_p = [&](const f32x16& sm, float m, u32x4& da, u32x4& db, u32x4& ua, u32x4& ub) {
      f32x16 e;
#pragma unroll
      for (int r = 0; r < 16; ++r) e[r] = __builtin_amdgcn_exp2f(sm[r] - m);
      const u32x4 pa = __builtin_bit_cast(u32x4, pack8<0>(e)), pb = __builtin_bit_cast(u32x4, pack8<8>(e));
#pragma unroll
      for (int i = 0; i < 4; ++i) {
        const int ra = 2 * i, rb = 8 + 2 * i;
        const int ka = (ra & 3) + 8 * (ra >> 2) + 4 * h, kb = (rb & 3) + 8 * (rb >> 2) + 4 * h;
        const unsigned ma = (ka <= ql ? 0xFFFFu : 0u) | (ka + 1 <= ql ? 0xFFFF0000u : 0u);
        const unsigned mb = (kb <= ql ? 0xFFFFu : 0u) | (kb + 1 <= ql ? 0xFFFF0000u : 0u);
        da[i] = pa[i] & ma; ua[i] = pa[i] & ~ma;
        db[i] = pb[i] & mb; ub[i] = pb[i] & ~mb;
      }
    };
    f32x16 o = zero16();
    if (chunk > 0) {
      bf16x8 kf[9];
      kf[8] = lds_read16(kbase + 8 * 1024);
      kf[0] = lds_read16(kbase);
      kf[1] = lds_read16(kbase + 1024);
      kf[2] = lds_read16(kbase + 2048);
      f32x16 sm = MFMA32(kf[8], qf, zero16());
      f32x16 s0 = MFMA32(kf[0], qf, zero16());
      f32x16 sc = MFMA32(kf[1], qf, zero16());
#pragma unroll
      for (int r = 0; r < 16; ++r) {
        const int kl = (r & 3) + 8 * (r >> 2) + 4 * h;
        sm[r] = (kl <= ql) ? sm[r] : s0[r];
      }
      float m = sm[0];
#pragma unroll
      for (int r = 1; r < 16; ++r) m = fmaxf(m, sm[r]);
#pragma unroll
      for (int i = 1; i < 8; ++i) {
        f32x16 sn;
        if (i + 2 < 8) kf[i + 2] = lds_read16(kbase + (i + 2) * 1024);
        if (i < 7) sn = MFMA32(kf[i + 1], qf, zero16());
        __builtin_amdgcn_sched_barrier(0);
#pragma unroll
        for (int r = 0; r < 16; r += 2) m = fmaxf(fmaxf(m, sc[r]), sc[r + 1]);
        __builtin_amdgcn_sched_barrier(0);
        if (i < 7) sc = sn;
      }
      m = swap_max(m);
      a_fetch();
      f32x16 negm;
#pragma unroll
      for (int r = 0; r < 16; ++r) negm[r] = -m;
      sc = MFMA32(kf[1], qf, negm);
      {
        const bf16x8 vd0 = lds_read16(vbase + 8 * vinc), vd1 = lds_read16(vbase + 8 * vinc + 512);
        const bf16x8 vu0 = lds_read16(vbase), vu1 = lds_read16(vbase + 512);
        u32x4 da, db, ua, ub;
        boundary_p(sm, m, da, db, ua, ub);
        o = MFMA32(vd0, __builtin_bit_cast(bf16x8, da), o);
        o = MFMA32(vd1, __builtin_bit_cast(bf16x8, db), o);
        o = MFMA32(vu0, __builtin_bit_cast(bf16x8, ua), o);
        o = MFMA32(vu1, __builtin_bit_cast(bf16x8, ub), o);
      }
#pragma unroll
      for (int i = 1; i < 8; ++i) {
        const bf16x8 v0 = lds_read16(vbase + i * vinc), v1 = lds_read16(vbase + i * vinc + 512);
        f32x16 sn, e;
        __builtin_amdgcn_sched_barrier(0);
#pragma unroll
        for (int r = 0; r < 4; ++r) e[r] = __builtin_amdgcn_exp2f(sc[r]);
        __builtin_amdgcn_sched_barrier(0);
        if (i < 7) sn = MFMA32(kf[i + 1], qf, negm);
        __builtin_amdgcn_sched_barrier(0);
#pragma unroll
        for (int r = 4; r < 8; ++r) e[r] = __builtin_amdgcn_exp2f(sc[r]);
        const bf16x8 p0 = pack8<0>(e);
#pragma unroll
        for (int r = 8; r < 10; ++r) e[r] = __builtin_amdgcn_exp2f(sc[r]);
        __builtin_amdgcn_sched_barrier(0);
        o = MFMA32(v0, p0, o);
        __builtin_amdgcn_sched_barrier(0);
#pragma unroll
        for (int r = 10; r < 16; ++r) e[r] = __builtin_amdgcn_exp2f(sc[r]);
        const bf16x8 p1 = pack8<8>(e);
        __builtin_amdgcn_sched_barrier(0);
        o = MFMA32(v1, p1, o);
        __builtin_amdgcn_sched_barrier(0);
        if (i < 7) sc = sn;
        if (i == A_AT) { a_readout(); __builtin_amdgcn_sched_barrier(0); }
      }
    } else {
      f32x16 sm = MFMA32(lds_read16(kbase + 8 * 1024), qf, zero16());
#pragma unroll
      for (int r = 0; r < 16; ++r) {
        const int kl = (r & 3) + 8 * (r >> 2) + 4 * h;
        sm[r] = (kl <= ql) ? sm[r] : -INFINITY;
      }
      float m = sm[0];
#pragma unroll
      for (int r = 1; r < 16; ++r) m = fmaxf(m, sm[r]);
#pragma unroll 1
      for (int i = first; i < 8; ++i) {
        const f32x16 s = MFMA32(lds_read16(kbase + i * 1024), qf, zero16());
#pragma unroll
        for (int r = 0; r < 16; r += 2) m = fmaxf(fmaxf(m, s[r]), s[r + 1]);
      }
      m = swap_max(m);
      a_fetch();
      u32x4 da, db, ua, ub;
      boundary_p(sm, m, da, db, ua, ub);
      o = MFMA32(lds_read16(vbase + 8 * vinc), __builtin_bit_cast(bf16x8, da), o);
      o = MFMA32(lds_read16(vbase + 8 * vinc + 512), __builtin_bit_cast(bf16x8, db), o);
#pragma unroll 1
      for (int i = first; i < 8; ++i) {
        const f32x16 s = MFMA32(lds_read16(kbase + i * 1024), qf, zero16());
        f32x16 e;
#pragma unroll
        for (int r = 0; r < 16; ++r) e[r] = __builtin_amdgcn_exp2f(s[r] - m);
        o = MFMA32(lds_read16(vbase + i * vinc), pack8<0>(e), o);
        o = MFMA32(lds_read16(vbase + i * vinc + 512), pack8<8>(e), o);
      }
      a_readout();
    }
    const float inv = __builtin_amdgcn_rcpf(o[8]);
    st_sc1_frag(wsr, WS_O + FRAG_BYTES + (head * NT + qt) * 1024 + lane * 16, pack8s<0>(o, inv));
    STAMP(6);
    STAMPW(16);
    drain_barrier();
    STAMP(9);
    if (w == 0) {
      signal_slots(ws, pslot, pval, lane >= 32);
      wait_slots(ws, cslot, cwant, lane >= 32);
    }
    wg_barrier();
    STAMP(10);
  }

  {
    const int tt = w & 3, kh = w >> 2;
    const int tile32 = ti3 * 4 + tt;
    const uint32_t og = WS_O + br3 * FRAG_BYTES + ((16 * kh) * NT + tile32) * 1024 + lane * 16;
    bf16x8 of[16];
#pragma unroll
    for (int i = 0; i < 16; ++i) of[i] = ld_sc1_frag(wsr, og + i * NT * 1024);
    __builtin_amdgcn_sched_barrier(0);
    const int blk = (lane >> 4) & 1, q4 = (lane & 15) >> 2, p4 = lane & 3;
    f32x16 acc0 = zero16(), acc1 = zero16();
    const char* wl = smem + L_WO;
    const int ra0 = (256 * kh + 4 * h + q4) * 128 + (((2 * blk + (p4 >> 1)) ^ (((q4 >> 1) & 1) << 2)) * 16) + 8 * (p4 & 1);
    const int ra1 = (256 * kh + 4 * h + q4) * 128 + (((4 + 2 * blk + (p4 >> 1)) ^ (((q4 >> 1) & 1) << 2)) * 16) + 8 * (p4 & 1);
#pragma unroll
    for (int i = 0; i < 16; ++i) {
      const int kb = 16 * i * 128;
      const bf16x8 a0 = tr_frag(wl + ra0 + kb, wl + ra0 + kb + 1024);
      const bf16x8 a1 = tr_frag(wl + ra1 + kb, wl + ra1 + kb + 1024);
      acc0 = MFMA32(a0, of[i], acc0);
      acc1 = MFMA32(a1, of[i], acc1);
    }
    STAMP(11);
    float* ex = (float*)smem + w * 1024 + lane * 4;
    const f32x16 give = kh ? acc0 : acc1;
    f32x16 keep = kh ? acc1 : acc0;
#pragma unroll
    for (int r4 = 0; r4 < 4; ++r4) {
      f32x4 v = {give[4 * r4], give[4 * r4 + 1], give[4 * r4 + 2], give[4 * r4 + 3]};
      *(f32x4*)(ex + r4 * 256) = v;
    }
    wg_barrier();
    const float* px = (const float*)smem + (w ^ 4) * 1024 + lane * 4;
#pragma unroll
    for (int r4 = 0; r4 < 4; ++r4) {
      const f32x4 v = *(const f32x4*)(px + r4 * 256);
#pragma unroll
      for (int c = 0; c < 4; ++c) keep[4 * r4 + c] += v[c];
    }
    const u32x4 lo = __builtin_bit_cast(u32x4, pack8<0>(keep)), hi = __builtin_bit_cast(u32x4, pack8<8>(keep));
    u16* op = out + (size_t)(tile32 * 32 + (lane & 31)) * 1024 + br3 * 512 + nj * 64 + kh * 32 + h * 8;
    auto t0 = __builtin_amdgcn_permlane32_swap(lo[0], lo[2], false, false);
    auto t1 = __builtin_amdgcn_permlane32_swap(lo[1], lo[3], false, false);
    u32x4 v = {t0[0], t1[0], t0[1], t1[1]};
    *(u32x4*)op = v;
    auto t2 = __builtin_amdgcn_permlane32_swap(hi[0], hi[2], false, false);
    auto t3 = __builtin_amdgcn_permlane32_swap(hi[1], hi[3], false, false);
    u32x4 u = {t2[0], t3[0], t2[1], t3[1]};
    *(u32x4*)(op + 16) = u;
  }
  STAMP(12);
}

extern "C" void kernel_launch(void* const* d_in, const int* in_sizes, int n_in, void* d_out, int out_size,
                              void* d_ws, size_t ws_size, hipStream_t stream) {
  fused<<<256, 512, 0, stream>>>((const u16*)d_in[0], (const u16*)d_in[1], (const u16*)d_in[2], (const u16*)d_in[3],
                                 (const u16*)d_in[4], (const u16*)d_in[5], (const u16*)d_in[6], (char*)d_ws, (u16*)d_out);
}
```

```cpp
#include <hip/hip_runtime.h>
#include <hip/hip_bf16.h>
#include <stdint.h>

typedef __bf16 bf16x8 __attribute__((ext_vector_type(8)));
typedef __bf16 bf16x4 __attribute__((ext_vector_type(4)));
typedef short s16x4 __attribute__((ext_vector_type(4)));
typedef short s16x8 __attribute__((ext_vector_type(8)));
typedef float f32x16 __attribute__((ext_vector_type(16)));
typedef float f32x4 __attribute__((ext_vector_type(4)));
typedef unsigned u32x4 __attribute__((ext_vector_type(4)));
typedef unsigned u32x2 __attribute__((ext_vector_type(2)));
typedef unsigned short u16;

#define LDS_AS __attribute__((address_space(3)))
#define GLB_AS __attribute__((address_space(1)))
#define DEVI __device__ __forceinline__
#define MFMA32(a, b, c) __builtin_amdgcn_mfma_f32_32x32x16_bf16((a), (b), (c), 0, 0, 0)

#define STAMPW(i)
#define STAMP(i)
constexpr int SEQ = 2048, DM = 512, NH = 32, NT = 64;
constexpr float L2E = 1.4426950408889634f;

constexpr uint32_t FRAG_BYTES = NH * NT * 1024;
constexpr uint32_t WS_QA = 0, WS_QB = FRAG_BYTES, WS_KB = 2 * FRAG_BYTES, WS_VB = 3 * FRAG_BYTES;
constexpr uint32_t WS_O = 4 * FRAG_BYTES;
constexpr uint32_t WS_KVP = 6 * FRAG_BYTES;
constexpr uint32_t KVP_BYTES = 1088;
constexpr uint32_t WS_E = WS_KVP + NH * 16 * KVP_BYTES;
constexpr uint32_t WS_F = WS_E + 256 * 64 * 8;
constexpr uint32_t WS_END = WS_F + 256 * 64 * 8;

DEVI void glds16s(const void* sbase, uint32_t voff, uint32_t lds_addr) {
  asm volatile("s_mov_b32 m0, %2\n\ts_nop 0\n\tglobal_load_lds_dwordx4 %0, %1" ::"v"(voff), "s"(sbase), "s"(lds_addr) : "memory");
}
DEVI void glds4s(const void* sbase, uint32_t voff, uint32_t lds_addr) {
  asm volatile("s_mov_b32 m0, %2\n\ts_nop 0\n\tglobal_load_lds_dword %0, %1" ::"v"(voff), "s"(sbase), "s"(lds_addr) : "memory");
}
DEVI uint32_t lds_addr_of(const char* p) { return (uint32_t)(uintptr_t)(const LDS_AS char*)p; }
DEVI bf16x8 lds_read16(const char* p) { return *(const bf16x8*)p; }
DEVI bf16x8 tr_frag(const char* p0, const char* p1) {
  s16x4 a = __builtin_amdgcn_ds_read_tr16_b64_v4i16((LDS_AS s16x4*)p0);
  s16x4 b = __builtin_amdgcn_ds_read_tr16_b64_v4i16((LDS_AS s16x4*)p1);
  s16x8 r = __builtin_shufflevector(a, b, 0, 1, 2, 3, 4, 5, 6, 7);
  return __builtin_bit_cast(bf16x8, r);
}
DEVI float max2(float a, float b) { return __builtin_elementwise_maximum(a, b); }
DEVI float max3(float a, float b, float c) { return max2(max2(a, b), c); }
DEVI float swap_max(float x) {
  auto t = __builtin_amdgcn_permlane32_swap(__float_as_uint(x), __float_as_uint(x), false, false);
  return max2(__uint_as_float(t[0]), __uint_as_float(t[1]));
}
DEVI float swap_add(float x) {
  auto t = __builtin_amdgcn_permlane32_swap(__float_as_uint(x), __float_as_uint(x), false, false);
  return __uint_as_float(t[0]) + __uint_as_float(t[1]);
}
typedef float f32x2 __attribute__((ext_vector_type(2)));
typedef __bf16 bf16x2 __attribute__((ext_vector_type(2)));
DEVI unsigned cvt2(float a, float b) {
  const f32x2 v = {a, b};
  return __builtin_bit_cast(unsigned, __builtin_convertvector(v, bf16x2));
}
template <int O>
DEVI bf16x8 pack8(const f32x16& a) {
  const u32x4 r = {cvt2(a[O], a[O + 1]), cvt2(a[O + 2], a[O + 3]), cvt2(a[O + 4], a[O + 5]), cvt2(a[O + 6], a[O + 7])};
  return __builtin_bit_cast(bf16x8, r);
}
template <int O>
DEVI bf16x8 pack8s(const f32x16& a, float s) {
  const u32x4 r = {cvt2(a[O] * s, a[O + 1] * s), cvt2(a[O + 2] * s, a[O + 3] * s), cvt2(a[O + 4] * s, a[O + 5] * s),
                   cvt2(a[O + 6] * s, a[O + 7] * s)};
  return __builtin_bit_cast(bf16x8, r);
}
DEVI f32x16 zero16() {
  f32x16 z;
#pragma unroll
  for (int i = 0; i < 16; ++i) z[i] = 0.f;
  return z;
}
DEVI bf16x8 wphi_frag_lds(const char* wl, int lane) {
  const char* p = wl + (4 * (lane >> 5) + ((lane & 15) >> 2)) * 32 + (lane & 3) * 8;
  const u32x4 r = __builtin_bit_cast(u32x4, tr_frag(p, p + 256));
  const unsigned sgn = (lane & 16) ? 0x80008000u : 0u;
  const u32x4 o = {r[0] ^ sgn, r[1] ^ sgn, r[2] ^ sgn, r[3] ^ sgn};
  return __builtin_bit_cast(bf16x8, o);
}
DEVI void dual_softmax(const f32x16& p, bf16x8& fp, bf16x8& fn) {
  float mp = max3(max3(p[0], p[1], p[2]), max3(p[3], p[4], p[5]), max2(p[6], p[7]));
  float mn = max3(max3(p[8], p[9], p[10]), max3(p[11], p[12], p[13]), max2(p[14], p[15]));
  mp = swap_max(mp) * L2E;
  mn = swap_max(mn) * L2E;
  f32x16 e;
  float sp = 0.f, sn = 0.f;
#pragma unroll
  for (int j = 0; j < 8; ++j) {
    e[j] = __builtin_amdgcn_exp2f(__builtin_fmaf(p[j], L2E, -mp));
    e[8 + j] = __builtin_amdgcn_exp2f(__builtin_fmaf(p[8 + j], L2E, -mn));
    sp += e[j];
    sn += e[8 + j];
  }
  sp = __builtin_amdgcn_rcpf(swap_add(sp));
  sn = __builtin_amdgcn_rcpf(swap_add(sn));
  fp = pack8s<0>(e, sp);
  fn = pack8s<8>(e, sn);
}

typedef unsigned long long u64;
typedef GLB_AS u64 gu64;
#define RLX_AGENT __ATOMIC_RELAXED, __HIP_MEMORY_SCOPE_AGENT
constexpr int AUX_SC1 = 16;
DEVI u32x4 ld_sc1(__amdgpu_buffer_rsrc_t r, uint32_t off) { return __builtin_amdgcn_raw_buffer_load_b128(r, off, 0, AUX_SC1); }
DEVI bf16x8 ld_sc1_frag(__amdgpu_buffer_rsrc_t r, uint32_t off) { return __builtin_bit_cast(bf16x8, ld_sc1(r, off)); }
DEVI void st_sc1(__amdgpu_buffer_rsrc_t r, uint32_t off, u32x4 v) { __builtin_amdgcn_raw_buffer_store_b128(v, r, off, 0, AUX_SC1); }
DEVI void st_sc1_frag(__amdgpu_buffer_rsrc_t r, uint32_t off, bf16x8 v) { st_sc1(r, off, __builtin_bit_cast(u32x4, v)); }
DEVI void wg_barrier() {
  asm volatile("s_waitcnt lgkmcnt(0)" ::: "memory");
  __builtin_amdgcn_s_barrier();
  asm volatile("" ::: "memory");
}
DEVI void drain_barrier() {
  asm volatile("s_waitcnt vmcnt(0) lgkmcnt(0)" ::: "memory");
  __builtin_amdgcn_s_barrier();
  asm volatile("" ::: "memory");
}
DEVI u64 mix(u64 e) { return e * 0x9E3779B97F4A7C15ull + 0x632BE59BD9B4E019ull; }
DEVI void wait_slots(char* ws, uint32_t slot, u64 want, bool active) {
  const gu64* f = (const gu64*)(ws + WS_F) + slot;
#pragma unroll 1
  for (int it = 0; it < 200000; ++it) {
    const u64 v = active ? __hip_atomic_load(f, RLX_AGENT) : want;
    if (__builtin_amdgcn_ballot_w64(v != want) == 0) break;
    __builtin_amdgcn_s_sleep(1);
  }
  if (active) __hip_atomic_store((gu64*)(ws + WS_E) + slot, want, RLX_AGENT);
}
DEVI void spin_slots(char* ws, uint32_t slot, u64 want, bool active) {
  const gu64* f = (const gu64*)(ws + WS_F) + slot;
#pragma unroll 1
  for (int it = 0; it < 200000; ++it) {
    const u64 v = active ? __hip_atomic_load(f, RLX_AGENT) : want;
    if (__builtin_amdgcn_ballot_w64(v != want) == 0) break;
    __builtin_amdgcn_s_sleep(1);
  }
}
DEVI void signal_slots(char* ws, uint32_t slot, u64 val, bool active) {
  if (active) __hip_atomic_store((gu64*)(ws + WS_F) + slot, val, RLX_AGENT);
}

#define OUTPOL 0
#define OUT_ST(p, v) (*(u32x4*)(p) = (v))
constexpr uint32_t CODE_PF = 20480;
constexpr int STAGE_BYTES = 40960;
constexpr int L_WPHI = 3 * STAGE_BYTES;
constexpr int L_K = 0, L_V = 16384, L_ONES = 32768, L_RED = 33792, L_KVF = L_RED + 17408, L_WO = 57344;
#define A_AT 3
constexpr int L_WPHI2 = L_WPHI + 2048;
constexpr int L_PF = L_WPHI2 + 1024;
constexpr int LDS_BYTES = L_PF + 256;

__global__ __launch_bounds__(512, 2) void fused(const u16* __restrict__ xA, const u16* __restrict__ wA,
                                                const u16* __restrict__ wphi, const u16* __restrict__ woA,
                                                const u16* __restrict__ xB, const u16* __restrict__ wB,
                                                const u16* __restrict__ woB, char* __restrict__ ws,
                                                u16* __restrict__ out) {
  __shared__ __attribute__((aligned(16))) char smem[LDS_BYTES];
  {
    const char* pc;
    asm volatile("s_getpc_b64 %0" : "=s"(pc));
    const uint32_t o = threadIdx.x * 64u;
    glds4s(pc, o < CODE_PF ? o : 0u, lds_addr_of(smem) + L_PF);
  }
  const int tid = threadIdx.x, lane = tid & 63;
  const int w = __builtin_amdgcn_readfirstlane(tid >> 6);
  const int bid = blockIdx.x, xcd = bid & 7, idx = bid >> 3;
  const int h = lane >> 5;
  const int br1 = xcd >> 2, jx = xcd & 3;
  const int ti1 = (jx & 1) * 8 + (idx & 7);
  const int g1 = (jx >> 1) * 4 + (idx >> 3);
  const int head = bid & 31, chunk = bid >> 5;
  const int br3 = xcd >> 2, ti3 = (xcd & 3) * 4 + (idx & 3), nj = idx >> 2;
  const __amdgpu_buffer_rsrc_t wsr = __builtin_amdgcn_make_buffer_rsrc(ws, 0, WS_END, 0x00020000);
  const uint32_t sm0 = lds_addr_of(smem);

  STAMP(0);
  const uint32_t cslot = bid * 64 + lane;
  uint32_t pslot;
  bool pact;
  {
    const int cc = (ti1 >> 1) + (lane >> 2);
    const int j = lane - 32, tj = 2 * chunk + ((j >> 3) & 1);
    const int c3 = ((j & 7) * 4 + (tj & 3)) * 8 + (j >> 4) * 4 + (tj >> 2);
    if (lane >= 32) { pact = true; pslot = c3 * 64 + 32 + head; }
    else if (br1) { pact = lane < 8 && cc <= 7; pslot = (cc * 32 + 4 * g1 + (lane & 3)) * 64 + (ti1 - 2 * cc + 2); }
    else { pact = true; pslot = ((lane >> 2) * 32 + 4 * g1 + (lane & 3)) * 64 + 16 + ti1; }
    if (!pact) pslot = cslot;
  }
  u64 cwant = 0, pval = 0;

  {
    const u16* x = br1 ? xB : xA;
    const u16* W = br1 ? wB : wA;
    const int tt = w & 3, hp = w >> 2;
    const uint32_t xo0 = (uint32_t)((ti1 * 128 + 16 * w + (lane >> 3)) * DM + (((lane & 7) ^ ((lane >> 4) & 7)) * 8)) * 2u;
    const uint32_t xo1 = (uint32_t)((ti1 * 128 + 16 * w + 8 + (lane >> 3)) * DM + (((lane & 7) ^ ((4 + (lane >> 4)) & 7)) * 8)) * 2u;
    const uint32_t wo0 = (uint32_t)((8 * w + (lane >> 3)) * 1536 + g1 * 64 + (((lane & 7) ^ (((lane >> 4) & 1) << 2)) * 8)) * 2u;
    const int s0 = ((idx & 7) + 2 * (idx >> 3)) & 7;
    auto stage_load = [&](int su, int buf, int part) {
      const int s = (su + s0) & 7;
      const uint32_t b = sm0 + buf * STAGE_BYTES;
      if (part == 0) {
        glds16s(x + s * 64, xo0, b + w * 2048);
        glds16s(x + s * 64, xo1, b + w * 2048 + 1024);
      } else if (part == 1) {
        glds16s(W + (size_t)s * 64 * 1536, wo0, b + 16384 + w * 1024);
        glds16s(W + (size_t)s * 64 * 1536 + 512, wo0, b + 16384 + 8192 + w * 1024);
      } else {
        glds16s(W + (size_t)s * 64 * 1536 + 1024, wo0, b + 16384 + 16384 + w * 1024);
      }
    };
    if (w == 1) glds16s(wphi + (size_t)(head & ~1) * 256, (uint32_t)lane * 16u, sm0 + L_WPHI2);
    if (br1 == 0 && w >= 6) glds16s(wphi + (size_t)(4 * g1) * 256 + (w - 6) * 512, (uint32_t)lane * 16u, sm0 + L_WPHI + (w - 6) * 1024);
    for (int p = 0; p < 3; ++p) stage_load(0, 0, p);
    for (int p = 0; p < 3; ++p) stage_load(1, 1, p);
    cwant = __hip_atomic_load((const gu64*)(ws + WS_E) + cslot, RLX_AGENT);
    pval = __hip_atomic_load((const gu64*)(ws + WS_E) + pslot, RLX_AGENT);

    const int xrow = 32 * tt + (lane & 31);
    const int xa0 = xrow * 128 + ((h ^ ((xrow >> 1) & 7)) * 16);
    const int q4 = (lane & 15) >> 2, p4 = lane & 3;
    const int wa0 = 16384 + (8 * h + q4) * 128 + (((4 * hp + 2 * ((lane >> 4) & 1) + (p4 >> 1)) ^ (((q4 >> 1) & 1) << 2)) * 16) + 8 * (p4 & 1);

    f32x16 aq = zero16(), ak = zero16(), av = zero16();
    bf16x8 xf[2], fq[2], fk[2], fv[2];
    auto frags = [&](const char* b, int ks, int p) {
      xf[p] = lds_read16(b + (xa0 ^ (32 * ks)));
      const char* wb = b + wa0 + ks * 2048;
      fq[p] = tr_frag(wb, wb + 512);
      fk[p] = tr_frag(wb + 8192, wb + 8192 + 512);
      fv[p] = tr_frag(wb + 16384, wb + 16384 + 512);
    };
    auto mma = [&](int p) {
      aq = MFMA32(fq[p], xf[p], aq);
      ak = MFMA32(fk[p], xf[p], ak);
      av = MFMA32(xf[p], fv[p], av);
    };
#pragma unroll
    for (int s = 0; s < 8; ++s) {
      if (s == 0) asm volatile("s_waitcnt vmcnt(7)" ::: "memory");
      else if (s < 7) asm volatile("s_waitcnt vmcnt(5)" ::: "memory");
      else asm volatile("s_waitcnt vmcnt(0)" ::: "memory");
      __builtin_amdgcn_s_barrier();
      asm volatile("" ::: "memory");
      if (s == 0) STAMP(1);
      const char* b = smem + (s % 3) * STAGE_BYTES;
      frags(b, 0, 0);
      if (s + 2 < 8) stage_load(s + 2, (s + 2) % 3, 0);
      __builtin_amdgcn_sched_barrier(0);
      if (s > 0) mma(1);
      __builtin_amdgcn_sched_barrier(0);
      frags(b, 1, 1);
      if (s + 2 < 8) stage_load(s + 2, (s + 2) % 3, 1);
      __builtin_amdgcn_sched_barrier(0);
      mma(0);
      __builtin_amdgcn_sched_barrier(0);
      frags(b, 2, 0);
      if (s + 2 < 8) stage_load(s + 2, (s + 2) % 3, 2);
      __builtin_amdgcn_sched_barrier(0);
      mma(1);
      __builtin_amdgcn_sched_barrier(0);
      frags(b, 3, 1);
      __builtin_amdgcn_sched_barrier(0);
      mma(0);
      __builtin_amdgcn_sched_barrier(0);
      asm volatile("s_waitcnt lgkmcnt(0)" ::: "memory");
    }
    mma(1);
    STAMP(2);
    const int tile32 = ti1 * 4 + tt, head0 = 4 * g1 + 2 * hp, blk = (lane >> 4) & 1;
    if (br1) {
      const uint32_t qo = (head0 * NT + tile32) * 1024 + lane * 16;
      st_sc1_frag(wsr, WS_QB + qo, pack8s<0>(aq, 0.25f * L2E));
      st_sc1_frag(wsr, WS_QB + qo + NT * 1024, pack8s<8>(aq, 0.25f * L2E));
      st_sc1_frag(wsr, WS_KB + qo, pack8<0>(ak));
      st_sc1_frag(wsr, WS_KB + qo + NT * 1024, pack8<8>(ak));
      const uint32_t vo = WS_VB + ((head0 + ((lane >> 4) & 1)) * NT + tile32) * 1024 + h * 256 + (lane & 15) * 16;
      st_sc1_frag(wsr, vo, pack8<0>(av));
      st_sc1_frag(wsr, vo + 512, pack8<8>(av));
    } else {
      const uint32_t qo = WS_QA + (head0 * NT + tile32) * 1024 + lane * 16;
      st_sc1_frag(wsr, qo, pack8<0>(aq));
      st_sc1_frag(wsr, qo + NT * 1024, pack8<8>(aq));
      s16x8 i0, i1;
#pragma unroll
      for (int j = 0; j < 8; ++j) {
        const int f = 8 * (j >> 2) + 4 * h + (j & 3);
        i0[j] = ((lane & 31) == f) ? (short)0x3F80 : (short)0;
        i1[j] = ((lane & 31) == 16 + f) ? (short)0x3F80 : (short)0;
      }
      const bf16x8 id0 = __builtin_bit_cast(bf16x8, i0), id1 = __builtin_bit_cast(bf16x8, i1);
      const u32x4 ones = {0x3F803F80u, 0x3F803F80u, 0x3F803F80u, 0x3F803F80u};
      const u32x4 vf0 = __builtin_bit_cast(u32x4, pack8<0>(av)), vf1 = __builtin_bit_cast(u32x4, pack8<8>(av));
      f32x16 pk0 = MFMA32(wphi_frag_lds(smem + L_WPHI + (2 * hp) * 512, lane), pack8<0>(ak), zero16());
      f32x16 pk1 = MFMA32(wphi_frag_lds(smem + L_WPHI + (2 * hp + 1) * 512, lane), pack8<8>(ak), zero16());
      STAMP(13);
      bf16x8 fp[2], fn[2];
      dual_softmax(pk0, fp[0], fn[0]);
      dual_softmax(pk1, fp[1], fn[1]);
      STAMP(14);
#pragma unroll
      for (int p = 0; p < 2; ++p) {
        f32x16 T = MFMA32(fp[p], id0, zero16());
        T = MFMA32(fn[p], id1, T);
        const bool mine = blk == p;
        f32x16 kvp = MFMA32(pack8<0>(T), __builtin_bit_cast(bf16x8, mine ? vf0 : ones), zero16());
        kvp = MFMA32(pack8<8>(T), __builtin_bit_cast(bf16x8, mine ? vf1 : ones), kvp);
        if (mine || (lane & 15) == 0) {
          char* dst = smem + ((2 * hp + p) * 4 + tt) * 2176 + (mine ? (h * 16 + (lane & 15)) * 16 : 2048 + h * 16);
          const int st = mine ? 512 : 32;
#pragma unroll
          for (int r4 = 0; r4 < 4; ++r4) {
            f32x4 v = {kvp[4 * r4], kvp[4 * r4 + 1], kvp[4 * r4 + 2], kvp[4 * r4 + 3]};
            *(f32x4*)(dst + r4 * st) = v;
          }
        }
      }
      wg_barrier();
      STAMP(15);
      if (w < 4 || (w == 4 && lane < 16)) {
        const int hh = w < 4 ? w : (lane >> 2);
        const char* src = smem + hh * 4 * 2176 + (w < 4 ? (lane >> 5) * 1024 + (lane & 31) * 16 : 2048 + (lane & 3) * 32);
        const int d2 = w < 4 ? 512 : 16;
        f32x4 s0v = *(const f32x4*)src, s1v = *(const f32x4*)(src + d2);
#pragma unroll
        for (int t = 1; t < 4; ++t) {
          s0v += *(const f32x4*)(src + t * 2176);
          s1v += *(const f32x4*)(src + t * 2176 + d2);
        }
        bf16x8 o;
#pragma unroll
        for (int c = 0; c < 4; ++c) { o[c] = (__bf16)s0v[c]; o[4 + c] = (__bf16)s1v[c]; }
        st_sc1_frag(wsr, WS_KVP + ((4 * g1 + hh) * 16 + ti1) * KVP_BYTES + (w < 4 ? lane * 16 : 1024 + (lane & 3) * 16), o);
      }
    }
    drain_barrier();
    STAMP(3);
    cwant = mix(cwant);
    if (w == 0) {
      pval = mix(pval);
      signal_slots(ws, pslot, pval, pact && lane < 32);
      wait_slots(ws, cslot, cwant, lane < 4 && 2 * chunk - 2 + lane >= 0);
    }
    if (tid >= 448) {
      const u32x4 ones = {0x3F803F80u, 0x3F803F80u, 0x3F803F80u, 0x3F803F80u};
      *(u32x4*)(smem + L_ONES + (tid - 448) * 16) = ones;
    }
    wg_barrier();
    STAMP(4);
  }

  const int qt = chunk * 8 + w;
  const bool lower = (lane & 16) == 0;
  bf16x8 qf;
  {
    const int t0 = chunk * 8 - 8;
    u32x4 kv[4];
    if (t0 + 2 * w >= 0) {
      const uint32_t o = (head * NT + t0 + 2 * w) * 1024 + lane * 16;
      kv[0] = ld_sc1(wsr, WS_KB + o);
      kv[1] = ld_sc1(wsr, WS_KB + o + 1024);
      kv[2] = ld_sc1(wsr, WS_VB + o);
      kv[3] = ld_sc1(wsr, WS_VB + o + 1024);
    }
    qf = ld_sc1_frag(wsr, WS_QB + (head * NT + qt) * 1024 + lane * 16);
    if (t0 + 2 * w >= 0) {
      *(u32x4*)(smem + L_K + 2 * w * 1024 + lane * 16) = kv[0];
      *(u32x4*)(smem + L_K + 2 * w * 1024 + 1024 + lane * 16) = kv[1];
      *(u32x4*)(smem + L_V + 2 * w * 1024 + lane * 16) = kv[2];
      *(u32x4*)(smem + L_V + 2 * w * 1024 + 1024 + lane * 16) = kv[3];
    }
    wg_barrier();
    STAMP(5);
  }
  const bool aslot = lane >= 16 && lane < 32;
  const u64 early = __hip_atomic_load((const gu64*)(ws + WS_F) + cslot, RLX_AGENT);
  u32x4 pp[2], pz;
  bf16x8 qa;
  const u16* wo = br3 ? woB : woA;
  auto wo_load = [&]() {
    const uint32_t off = (uint32_t)((64 * w + (lane >> 3)) * DM + nj * 64 + (((lane & 7) ^ (((lane >> 4) & 1) << 2)) * 8)) * 2u;
#pragma unroll
    for (int i = 0; i < 8; ++i) glds16s(wo + i * 8 * DM, off, sm0 + L_WO + w * 8192 + i * 1024);
  };

  {
    const char* kbase = smem + L_K + w * 1024 + lane * 16;
    const int vinc = lower ? 1024 : 0;
    const char* vbase = lower ? (smem + L_V + w * 1024 + h * 256 + (lane & 15) * 16) : (smem + L_ONES + (lane & 15) * 16);
    const int ql = lane & 31;
    const int first = chunk > 0 ? 0 : 8 - w;
    auto a_fetch = [&]() {
      if (__builtin_amdgcn_ballot_w64(aslot && early != cwant) != 0) spin_slots(ws, cslot, cwant, aslot);
      asm volatile("" ::: "memory");
      if (w == 0 && aslot) __hip_atomic_store((gu64*)(ws + WS_E) + cslot, cwant, RLX_AGENT);
      const uint32_t kb = WS_KVP + (head * 16 + 2 * w) * KVP_BYTES;
      pp[0] = ld_sc1(wsr, kb + lane * 16);
      pp[1] = ld_sc1(wsr, kb + KVP_BYTES + lane * 16);
      pz = ld_sc1(wsr, kb + ((lane >> 2) & 1) * KVP_BYTES + 1024 + (lane & 3) * 16);
      qa = ld_sc1_frag(wsr, WS_QA + (head * NT + qt) * 1024 + lane * 16);
    };
    auto a_readout = [&]() {
      f32x4 s0v, s1v, z0, z1;
      s0v[0] = __uint_as_float(pp[0][0] << 16) + __uint_as_float(pp[1][0] << 16);
      s0v[1] = __uint_as_float(pp[0][0] & 0xFFFF0000u) + __uint_as_float(pp[1][0] & 0xFFFF0000u);
      s0v[2] = __uint_as_float(pp[0][1] << 16) + __uint_as_float(pp[1][1] << 16);
      s0v[3] = __uint_as_float(pp[0][1] & 0xFFFF0000u) + __uint_as_float(pp[1][1] & 0xFFFF0000u);
      s1v[0] = __uint_as_float(pp[0][2] << 16) + __uint_as_float(pp[1][2] << 16);
      s1v[1] = __uint_as_float(pp[0][2] & 0xFFFF0000u) + __uint_as_float(pp[1][2] & 0xFFFF0000u);
      s1v[2] = __uint_as_float(pp[0][3] << 16) + __uint_as_float(pp[1][3] << 16);
      s1v[3] = __uint_as_float(pp[0][3] & 0xFFFF0000u) + __uint_as_float(pp[1][3] & 0xFFFF0000u);
      z0[0] = __uint_as_float(pz[0] << 16); z0[1] = __uint_as_float(pz[0] & 0xFFFF0000u);
      z0[2] = __uint_as_float(pz[1] << 16); z0[3] = __uint_as_float(pz[1] & 0xFFFF0000u);
      z1[0] = __uint_as_float(pz[2] << 16); z1[1] = __uint_as_float(pz[2] & 0xFFFF0000u);
      z1[2] = __uint_as_float(pz[3] << 16); z1[3] = __uint_as_float(pz[3] & 0xFFFF0000u);
#pragma unroll
      for (int c = 0; c < 4; ++c) { z0[c] += __shfl_xor(z0[c], 4); z1[c] += __shfl_xor(z1[c], 4); }
      char* red = smem + L_RED;
      *(f32x4*)(red + w * 2048 + lane * 16) = s0v;
      *(f32x4*)(red + w * 2048 + 1024 + lane * 16) = s1v;
      if (lane < 4) {
        *(f32x4*)(red + 16384 + w * 128 + lane * 32) = z0;
        *(f32x4*)(red + 16384 + w * 128 + lane * 32 + 16) = z1;
      }
      wo_load();
      const bf16x8 wf = wphi_frag_lds(smem + L_WPHI2 + (head & 1) * 512, lane);
      const f32x16 pq = MFMA32(wf, qa, zero16());
      wg_barrier();
      STAMP(7);
      if (w == 0) {
        const char* src = red + lane * 16;
        f32x4 t0 = *(const f32x4*)src, t1 = *(const f32x4*)(src + 1024);
#pragma unroll
        for (int t = 1; t < 8; ++t) { t0 += *(const f32x4*)(src + t * 2048); t1 += *(const f32x4*)(src + t * 2048 + 1024); }
        bf16x8 o;
#pragma unroll
        for (int c = 0; c < 4; ++c) { o[c] = (__bf16)t0[c]; o[4 + c] = (__bf16)t1[c]; }
        *(bf16x8*)(smem + L_KVF + (lane >> 5) * 1024 + (((lane >> 4) & 1) * 32 + (lane & 15)) * 16) = o;
      }
      if (w == 1) {
        const int f = lane & 31;
        const float* zr = (const float*)(red + 16384);
        const __bf16 z = (__bf16)(((zr[f] + zr[32 + f]) + (zr[64 + f] + zr[96 + f])) + ((zr[128 + f] + zr[160 + f]) + (zr[192 + f] + zr[224 + f])));
#pragma unroll
        for (int i = 0; i < 8; ++i)
          *(__bf16*)(smem + L_KVF + (f >> 4) * 1024 + (((f >> 2) & 1) * 32 + 16 + 8 * h + i) * 16 + (((f >> 3) & 1) * 4 + (f & 3)) * 2) = z;
      }
      bf16x8 fp, fn;
      dual_softmax(pq, fp, fn);
      wg_barrier();
      f32x16 oa = MFMA32(lds_read16(smem + L_KVF + lane * 16), fp, zero16());
      oa = MFMA32(lds_read16(smem + L_KVF + 1024 + lane * 16), fn, oa);
      const float inv = __builtin_amdgcn_rcpf(oa[8] + 1e-6f);
      st_sc1_frag(wsr, WS_O + (head * NT + qt) * 1024 + lane * 16, pack8s<0>(oa, inv));
      STAMP(8);
    };
    auto boundary_p = [&](const f32x16& sm, float m, u32x4& da, u32x4& db, u32x4& ua, u32x4& ub) {
      f32x16 e;
#pragma unroll
      for (int r = 0; r < 16; ++r) e[r] = __builtin_amdgcn_exp2f(sm[r] - m);
      const u32x4 pa = __builtin_bit_cast(u32x4, pack8<0>(e)), pb = __builtin_bit_cast(u32x4, pack8<8>(e));
#pragma unroll
      for (int i = 0; i < 4; ++i) {
        const int ra = 2 * i, rb = 8 + 2 * i;
        const int ka = (ra & 3) + 8 * (ra >> 2) + 4 * h, kb = (rb & 3) + 8 * (rb >> 2) + 4 * h;
        const unsigned ma = (ka <= ql ? 0xFFFFu : 0u) | (ka + 1 <= ql ? 0xFFFF0000u : 0u);
        const unsigned mb = (kb <= ql ? 0xFFFFu : 0u) | (kb + 1 <= ql ? 0xFFFF0000u : 0u);
        da[i] = pa[i] & ma; ua[i] = pa[i] & ~ma;
        db[i] = pb[i] & mb; ub[i] = pb[i] & ~mb;
      }
    };
    f32x16 o = zero16();
    if (chunk > 0) {
      bf16x8 kf[9];
      kf[8] = lds_read16(kbase + 8 * 1024);
      kf[0] = lds_read16(kbase);
      kf[1] = lds_read16(kbase + 1024);
      kf[2] = lds_read16(kbase + 2048);
      f32x16 sm = MFMA32(kf[8], qf, zero16());
      f32x16 s0 = MFMA32(kf[0], qf, zero16());
      f32x16 sc = MFMA32(kf[1], qf, zero16());
#pragma unroll
      for (int r = 0; r < 16; ++r) {
        const int kl = (r & 3) + 8 * (r >> 2) + 4 * h;
        sm[r] = (kl <= ql) ? sm[r] : s0[r];
      }
      float m = sm[0];
#pragma unroll
      for (int r = 1; r < 16; ++r) m = fmaxf(m, sm[r]);
#pragma unroll
      for (int i = 1; i < 8; ++i) {
        f32x16 sn;
        if (i + 2 < 8) kf[i + 2] = lds_read16(kbase + (i + 2) * 1024);
        if (i < 7) sn = MFMA32(kf[i + 1], qf, zero16());
        __builtin_amdgcn_sched_barrier(0);
#pragma unroll
        for (int r = 0; r < 16; r += 2) m = fmaxf(fmaxf(m, sc[r]), sc[r + 1]);
        __builtin_amdgcn_sched_barrier(0);
        if (i < 7) sc = sn;
      }
      m = swap_max(m);
      a_fetch();
      f32x16 negm;
#pragma unroll
      for (int r = 0; r < 16; ++r) negm[r] = -m;
      sc = MFMA32(kf[1], qf, negm);
      {
        const bf16x8 vd0 = lds_read16(vbase + 8 * vinc), vd1 = lds_read16(vbase + 8 * vinc + 512);
        const bf16x8 vu0 = lds_read16(vbase), vu1 = lds_read16(vbase + 512);
        u32x4 da, db, ua, ub;
        boundary_p(sm, m, da, db, ua, ub);
        o = MFMA32(vd0, __builtin_bit_cast(bf16x8, da), o);
        o = MFMA32(vd1, __builtin_bit_cast(bf16x8, db), o);
        o = MFMA32(vu0, __builtin_bit_cast(bf16x8, ua), o);
        o = MFMA32(vu1, __builtin_bit_cast(bf16x8, ub), o);
      }
#pragma unroll
      for (int i = 1; i < 8; ++i) {
        const bf16x8 v0 = lds_read16(vbase + i * vinc), v1 = lds_read16(vbase + i * vinc + 512);
        f32x16 sn, e;
        __builtin_amdgcn_sched_barrier(0);
#pragma unroll
        for (int r = 0; r < 4; ++r) e[r] = __builtin_amdgcn_exp2f(sc[r]);
        __builtin_amdgcn_sched_barrier(0);
        if (i < 7) sn = MFMA32(kf[i + 1], qf, negm);
        __builtin_amdgcn_sched_barrier(0);
#pragma unroll
        for (int r = 4; r < 8; ++r) e[r] = __builtin_amdgcn_exp2f(sc[r]);
        const bf16x8 p0 = pack8<0>(e);
#pragma unroll
        for (int r = 8; r < 10; ++r) e[r] = __builtin_amdgcn_exp2f(sc[r]);
        __builtin_amdgcn_sched_barrier(0);
        o = MFMA32(v0, p0, o);
        __builtin_amdgcn_sched_barrier(0);
#pragma unroll
        for (int r = 10; r < 16; ++r) e[r] = __builtin_amdgcn_exp2f(sc[r]);
        const bf16x8 p1 = pack8<8>(e);
        __builtin_amdgcn_sched_barrier(0);
        o = MFMA32(v1, p1, o);
        __builtin_amdgcn_sched_barrier(0);
        if (i < 7) sc = sn;
        if (i == A_AT) { a_readout(); __builtin_amdgcn_sched_barrier(0); }
      }
    } else {
      f32x16 sm = MFMA32(lds_read16(kbase + 8 * 1024), qf, zero16());
#pragma unroll
      for (int r = 0; r < 16; ++r) {
        const int kl = (r & 3) + 8 * (r >> 2) + 4 * h;
        sm[r] = (kl <= ql) ? sm[r] : -INFINITY;
      }
      float m = sm[0];
#pragma unroll
      for (int r = 1; r < 16; ++r) m = fmaxf(m, sm[r]);
#pragma unroll 1
      for (int i = first; i < 8; ++i) {
        const f32x16 s = MFMA32(lds_read16(kbase + i * 1024), qf, zero16());
#pragma unroll
        for (int r = 0; r < 16; r += 2) m = fmaxf(fmaxf(m, s[r]), s[r + 1]);
      }
      m = swap_max(m);
      a_fetch();
      u32x4 da, db, ua, ub;
      boundary_p(sm, m, da, db, ua, ub);
      o = MFMA32(lds_read16(vbase + 8 * vinc), __builtin_bit_cast(bf16x8, da), o);
      o = MFMA32(lds_read16(vbase + 8 * vinc + 512), __builtin_bit_cast(bf16x8, db), o);
#pragma unroll 1
      for (int i = first; i < 8; ++i) {
        const f32x16 s = MFMA32(lds_read16(kbase + i * 1024), qf, zero16());
        f32x16 e;
#pragma unroll
        for (int r = 0; r < 16; ++r) e[r] = __builtin_amdgcn_exp2f(s[r] - m);
        o = MFMA32(lds_read16(vbase + i * vinc), pack8<0>(e), o);
        o = MFMA32(lds_read16(vbase + i * vinc + 512), pack8<8>(e), o);
      }
      a_readout();
    }
    const float inv = __builtin_amdgcn_rcpf(o[8]);
    st_sc1_frag(wsr, WS_O + FRAG_BYTES + (head * NT + qt) * 1024 + lane * 16, pack8s<0>(o, inv));
    STAMP(6);
    STAMPW(16);
    drain_barrier();
    STAMP(9);
    if (w == 0) {
      signal_slots(ws, pslot, pval, lane >= 32);
      wait_slots(ws, cslot, cwant, lane >= 32);
    }
    wg_barrier();
    STAMP(10);
  }

  {
    const int tt = w & 3, kh = w >> 2;
    const int tile32 = ti3 * 4 + tt;
    const uint32_t og = WS_O + br3 * FRAG_BYTES + ((16 * kh) * NT + tile32) * 1024 + lane * 16;
    bf16x8 of[16];
#pragma unroll
    for (int i = 0; i < 16; ++i) of[i] = ld_sc1_frag(wsr, og + i * NT * 1024);
    __builtin_amdgcn_sched_barrier(0);
    const int blk = (lane >> 4) & 1, q4 = (lane & 15) >> 2, p4 = lane & 3;
    f32x16 acc0 = zero16(), acc1 = zero16();
    const char* wl = smem + L_WO;
    const int ra0 = (256 * kh + 4 * h + q4) * 128 + (((2 * blk + (p4 >> 1)) ^ (((q4 >> 1) & 1) << 2)) * 16) + 8 * (p4 & 1);
    const int ra1 = (256 * kh + 4 * h + q4) * 128 + (((4 + 2 * blk + (p4 >> 1)) ^ (((q4 >> 1) & 1) << 2)) * 16) + 8 * (p4 & 1);
#pragma unroll
    for (int i = 0; i < 16; ++i) {
      const int kb = 16 * i * 128;
      const bf16x8 a0 = tr_frag(wl + ra0 + kb, wl + ra0 + kb + 1024);
      const bf16x8 a1 = tr_frag(wl + ra1 + kb, wl + ra1 + kb + 1024);
      acc0 = MFMA32(a0, of[i], acc0);
      acc1 = MFMA32(a1, of[i], acc1);
    }
    STAMP(11);
    float* ex = (float*)smem + w * 1024 + lane * 4;
    const f32x16 give = kh ? acc0 : acc1;
    f32x16 keep = kh ? acc1 : acc0;
#pragma unroll
    for (int r4 = 0; r4 < 4; ++r4) {
      f32x4 v = {give[4 * r4], give[4 * r4 + 1], give[4 * r4 + 2], give[4 * r4 + 3]};
      *(f32x4*)(ex + r4 * 256) = v;
    }
    wg_barrier();
    const float* px = (const float*)smem + (w ^ 4) * 1024 + lane * 4;
#pragma unroll
    for (int r4 = 0; r4 < 4; ++r4) {
      const f32x4 v = *(const f32x4*)(px + r4 * 256);
#pragma unroll
      for (int c = 0; c < 4; ++c) keep[4 * r4 + c] += v[c];
    }
    const u32x4 lo = __builtin_bit_cast(u32x4, pack8<0>(keep)), hi = __builtin_bit_cast(u32x4, pack8<8>(keep));
    u16* op = out + (size_t)(tile32 * 32 + (lane & 31)) * 1024 + br3 * 512 + nj * 64 + kh * 32 + h * 8;
    auto t0 = __builtin_amdgcn_permlane32_swap(lo[0], lo[2], false, false);
    auto t1 = __builtin_amdgcn_permlane32_swap(lo[1], lo[3], false, false);
    u32x4 v = {t0[0], t1[0], t0[1], t1[1]};
    OUT_ST(op, v);
    auto t2 = __builtin_amdgcn_permlane32_swap(hi[0], hi[2], false, false);
    auto t3 = __builtin_amdgcn_permlane32_swap(hi[1], hi[3], false, false);
    u32x4 u = {t2[0], t3[0], t2[1], t3[1]};
    OUT_ST(op + 16, u);
  }
  STAMP(12);
}

extern "C" void kernel_launch(void* const* d_in, const int* in_sizes, int n_in, void* d_out, int out_size,
                              void* d_ws, size_t ws_size, hipStream_t stream) {
  fused<<<256, 512, 0, stream>>>((const u16*)d_in[0], (const u16*)d_in[1], (const u16*)d_in[2], (const u16*)d_in[3],
                                 (const u16*)d_in[4], (const u16*)d_in[5], (const u16*)d_in[6], (char*)d_ws, (u16*)d_out);
}
```

```cpp
#include <hip/hip_runtime.h>
#include <hip/hip_bf16.h>
#include <stdint.h>

typedef __bf16 bf16x8 __attribute__((ext_vector_type(8)));
typedef __bf16 bf16x4 __attribute__((ext_vector_type(4)));
typedef short s16x4 __attribute__((ext_vector_type(4)));
typedef short s16x8 __attribute__((ext_vector_type(8)));
typedef float f32x16 __attribute__((ext_vector_type(16)));
typedef float f32x4 __attribute__((ext_vector_type(4)));
typedef unsigned u32x4 __attribute__((ext_vector_type(4)));
typedef unsigned u32x2 __attribute__((ext_vector_type(2)));
typedef unsigned short u16;

#define LDS_AS __attribute__((address_space(3)))
#define GLB_AS __attribute__((address_space(1)))
#define DEVI __device__ __forceinline__
#define MFMA32(a, b, c) __builtin_amdgcn_mfma_f32_32x32x16_bf16((a), (b), (c), 0, 0, 0)

#define STAMPW(i)
#define STAMP(i)
constexpr int SEQ = 2048, DM = 512, NH = 32, NT = 64;
constexpr float L2E = 1.4426950408889634f;

constexpr uint32_t FRAG_BYTES = NH * NT * 1024;
constexpr uint32_t WS_QA = 0, WS_QB = FRAG_BYTES, WS_KB = 2 * FRAG_BYTES, WS_VB = 3 * FRAG_BYTES;
constexpr uint32_t WS_O = 4 * FRAG_BYTES;
constexpr uint32_t WS_KVP = 6 * FRAG_BYTES;
constexpr uint32_t KVP_BYTES = 1088;
constexpr uint32_t WS_E = WS_KVP + NH * 16 * KVP_BYTES;
constexpr uint32_t WS_F = WS_E + 256 * 64 * 8;
constexpr uint32_t WS_END = WS_F + 256 * 64 * 8;

DEVI void glds16s(const void* sbase, uint32_t voff, uint32_t lds_addr) {
  asm volatile("s_mov_b32 m0, %2\n\ts_nop 0\n\tglobal_load_lds_dwordx4 %0, %1" ::"v"(voff), "s"(sbase), "s"(lds_addr) : "memory");
}
DEVI void glds4s(const void* sbase, uint32_t voff, uint32_t lds_addr) {
  asm volatile("s_mov_b32 m0, %2\n\ts_nop 0\n\tglobal_load_lds_dword %0, %1" ::"v"(voff), "s"(sbase), "s"(lds_addr) : "memory");
}
DEVI uint32_t lds_addr_of(const char* p) { return (uint32_t)(uintptr_t)(const LDS_AS char*)p; }
DEVI bf16x8 lds_read16(const char* p) { return *(const bf16x8*)p; }
DEVI bf16x8 tr_frag(const char* p0, const char* p1) {
  s16x4 a = __builtin_amdgcn_ds_read_tr16_b64_v4i16((LDS_AS s16x4*)p0);
  s16x4 b = __builtin_amdgcn_ds_read_tr16_b64_v4i16((LDS_AS s16x4*)p1);
  s16x8 r = __builtin_shufflevector(a, b, 0, 1, 2, 3, 4, 5, 6, 7);
  return __builtin_bit_cast(bf16x8, r);
}
DEVI float max2(float a, float b) { return __builtin_elementwise_maximum(a, b); }
DEVI float max3(float a, float b, float c) { return max2(max2(a, b), c); }
DEVI float swap_max(float x) {
  auto t = __builtin_amdgcn_permlane32_swap(__float_as_uint(x), __float_as_uint(x), false, false);
  return max2(__uint_as_float(t[0]), __uint_as_float(t[1]));
}
DEVI float swap_add(float x) {
  auto t = __builtin_amdgcn_permlane32_swap(__float_as_uint(x), __float_as_uint(x), false, false);
  return __uint_as_float(t[0]) + __uint_as_float(t[1]);
}
typedef float f32x2 __attribute__((ext_vector_type(2)));
typedef __bf16 bf16x2 __attribute__((ext_vector_type(2)));
DEVI unsigned cvt2(float a, float b) {
  const f32x2 v = {a, b};
  return __builtin_bit_cast(unsigned, __builtin_convertvector(v, bf16x2));
}
template <int O>
DEVI bf16x8 pack8(const f32x16& a) {
  const u32x4 r = {cvt2(a[O], a[O + 1]), cvt2(a[O + 2], a[O + 3]), cvt2(a[O + 4], a[O + 5]), cvt2(a[O + 6], a[O + 7])};
  return __builtin_bit_cast(bf16x8, r);
}
template <int O>
DEVI bf16x8 pack8s(const f32x16& a, float s) {
  const u32x4 r = {cvt2(a[O] * s, a[O + 1] * s), cvt2(a[O + 2] * s, a[O + 3] * s), cvt2(a[O + 4] * s, a[O + 5] * s),
                   cvt2(a[O + 6] * s, a[O + 7] * s)};
  return __builtin_bit_cast(bf16x8, r);
}
DEVI f32x16 zero16() {
  f32x16 z;
#pragma unroll
  for (int i = 0; i < 16; ++i) z[i] = 0.f;
  return z;
}
DEVI bf16x8 wphi_frag_lds(const char* wl, int lane) {
  const char* p = wl + (4 * (lane >> 5) + ((lane & 15) >> 2)) * 32 + (lane & 3) * 8;
  const u32x4 r = __builtin_bit_cast(u32x4, tr_frag(p, p + 256));
  const unsigned sgn = (lane & 16) ? 0x80008000u : 0u;
  const u32x4 o = {r[0] ^ sgn, r[1] ^ sgn, r[2] ^ sgn, r[3] ^ sgn};
  return __builtin_bit_cast(bf16x8, o);
}
DEVI void dual_softmax(const f32x16& p, bf16x8& fp, bf16x8& fn) {
  float mp = max3(max3(p[0], p[1], p[2]), max3(p[3], p[4], p[5]), max2(p[6], p[7]));
  float mn = max3(max3(p[8], p[9], p[10]), max3(p[11], p[12], p[13]), max2(p[14], p[15]));
  mp = swap_max(mp) * L2E;
  mn = swap_max(mn) * L2E;
  f32x16 e;
  float sp = 0.f, sn = 0.f;
#pragma unroll
  for (int j = 0; j < 8; ++j) {
    e[j] = __builtin_amdgcn_exp2f(__builtin_fmaf(p[j], L2E, -mp));
    e[8 + j] = __builtin_amdgcn_exp2f(__builtin_fmaf(p[8 + j], L2E, -mn));
    sp += e[j];
    sn += e[8 + j];
  }
  sp = __builtin_amdgcn_rcpf(swap_add(sp));
  sn = __builtin_amdgcn_rcpf(swap_add(sn));
  fp = pack8s<0>(e, sp);
  fn = pack8s<8>(e, sn);
}

typedef unsigned long long u64;
typedef GLB_AS u64 gu64;
#define RLX_AGENT __ATOMIC_RELAXED, __HIP_MEMORY_SCOPE_AGENT
constexpr int AUX_SC1 = 16;
DEVI u32x4 ld_sc1(__amdgpu_buffer_rsrc_t r, uint32_t off) { return __builtin_amdgcn_raw_buffer_load_b128(r, off, 0, AUX_SC1); }
DEVI bf16x8 ld_sc1_frag(__amdgpu_buffer_rsrc_t r, uint32_t off) { return __builtin_bit_cast(bf16x8, ld_sc1(r, off)); }
DEVI void st_sc1(__amdgpu_buffer_rsrc_t r, uint32_t off, u32x4 v) { __builtin_amdgcn_raw_buffer_store_b128(v, r, off, 0, AUX_SC1); }
DEVI void st_sc1_frag(__amdgpu_buffer_rsrc_t r, uint32_t off, bf16x8 v) { st_sc1(r, off, __builtin_bit_cast(u32x4, v)); }
DEVI void wg_barrier() {
  asm volatile("s_waitcnt lgkmcnt(0)" ::: "memory");
  __builtin_amdgcn_s_barrier();
  asm volatile("" ::: "memory");
}
DEVI void drain_barrier() {
  asm volatile("s_waitcnt vmcnt(0) lgkmcnt(0)" ::: "memory");
  __builtin_amdgcn_s_barrier();
  asm volatile("" ::: "memory");
}
#define POLL_GAP 8
DEVI u64 mix(u64 e) { return e * 0x9E3779B97F4A7C15ull + 0x632BE59BD9B4E019ull; }
struct Polls { u64 a, b, c; };
DEVI u64 poll_slot(char* ws, uint32_t poll) { return __hip_atomic_load((const gu64*)(ws + WS_F) + poll, RLX_AGENT); }
#define POLL_ISSUE(r) asm volatile("global_load_dwordx2 %0, %1, %2 sc1" : "=v"(r) : "v"(off), "s"(base) : "memory")
#define POLL_WAIT(r) asm volatile("s_waitcnt vmcnt(2)" : "+v"(r)::"memory")
DEVI void wait_slots(char* ws, uint32_t poll, u64 want, bool active, u64 first, Polls& p) {
  if (__builtin_amdgcn_ballot_w64(active && first != want) != 0) {
    const char* base = ws + WS_F;
    const uint32_t off = poll * 8u;
    POLL_ISSUE(p.a);
    __builtin_amdgcn_s_sleep(POLL_GAP);
    POLL_ISSUE(p.b);
    __builtin_amdgcn_s_sleep(POLL_GAP);
    POLL_ISSUE(p.c);
#pragma unroll 1
    for (int it = 0; it < 100000; ++it) {
      POLL_WAIT(p.a);
      if (__builtin_amdgcn_ballot_w64(active && p.a != want) == 0) break;
      POLL_ISSUE(p.a);
      POLL_WAIT(p.b);
      if (__builtin_amdgcn_ballot_w64(active && p.b != want) == 0) break;
      POLL_ISSUE(p.b);
      POLL_WAIT(p.c);
      if (__builtin_amdgcn_ballot_w64(active && p.c != want) == 0) break;
      POLL_ISSUE(p.c);
    }
  }
  if (active) __hip_atomic_store((gu64*)(ws + WS_E) + poll, want, RLX_AGENT);
}
DEVI void poll_retire(Polls& p) { asm volatile("s_waitcnt vmcnt(0)" : "+v"(p.a), "+v"(p.b), "+v"(p.c)::"memory"); }
DEVI void spin_slots(char* ws, uint32_t slot, u64 want, bool active) {
  const gu64* f = (const gu64*)(ws + WS_F) + slot;
#pragma unroll 1
  for (int it = 0; it < 200000; ++it) {
    const u64 v = active ? __hip_atomic_load(f, RLX_AGENT) : want;
    if (__builtin_amdgcn_ballot_w64(v != want) == 0) break;
    __builtin_amdgcn_s_sleep(1);
  }
}
DEVI void signal_slots(char* ws, uint32_t slot, u64 val, bool active) {
  if (active) __hip_atomic_store((gu64*)(ws + WS_F) + slot, val, RLX_AGENT);
}

#define OUTPOL 0
#define OUT_ST(p, v) (*(u32x4*)(p) = (v))
constexpr uint32_t CODE_PF = 20992;
constexpr int STAGE_BYTES = 40960;
constexpr int L_WPHI = 3 * STAGE_BYTES;
constexpr int L_K = 0, L_V = 16384, L_ONES = 32768, L_RED = 33792, L_KVF = L_RED + 17408, L_WO = 57344;
#define A_AT 3
constexpr int L_WPHI2 = L_WPHI + 2048;
constexpr int L_PF = L_WPHI2 + 1024;
constexpr int LDS_BYTES = L_PF + 256;

__global__ __launch_bounds__(512, 2) void fused(const u16* __restrict__ xA, const u16* __restrict__ wA,
                                                const u16* __restrict__ wphi, const u16* __restrict__ woA,
                                                const u16* __restrict__ xB, const u16* __restrict__ wB,
                                                const u16* __restrict__ woB, char* __restrict__ ws,
                                                u16* __restrict__ out) {
  __shared__ __attribute__((aligned(16))) char smem[LDS_BYTES];
  {
    const char* pc;
    asm volatile("s_getpc_b64 %0" : "=s"(pc));
    const uint32_t o = threadIdx.x * 64u;
    glds4s(pc, o < CODE_PF ? o : 0u, lds_addr_of(smem) + L_PF);
  }
  const int tid = threadIdx.x, lane = tid & 63;
  const int w = __builtin_amdgcn_readfirstlane(tid >> 6);
  const int bid = blockIdx.x, xcd = bid & 7, idx = bid >> 3;
  const int h = lane >> 5;
  const int br1 = xcd >> 2, jx = xcd & 3;
  const int ti1 = (jx & 1) * 8 + (idx & 7);
  const int g1 = (jx >> 1) * 4 + (idx >> 3);
  const int head = bid & 31, chunk = bid >> 5;
  const int br3 = xcd >> 2, ti3 = (xcd & 3) * 4 + (idx & 3), nj = idx >> 2;
  const __amdgpu_buffer_rsrc_t wsr = __builtin_amdgcn_make_buffer_rsrc(ws, 0, WS_END, 0x00020000);
  const uint32_t sm0 = lds_addr_of(smem);

  STAMP(0);
  const uint32_t cslot = bid * 64 + lane;
  uint32_t pslot;
  bool pact;
  {
    const int cc = (ti1 >> 1) + (lane >> 2);
    const int j = lane - 32, tj = 2 * chunk + ((j >> 3) & 1);
    const int c3 = ((j & 7) * 4 + (tj & 3)) * 8 + (j >> 4) * 4 + (tj >> 2);
    if (lane >= 32) { pact = true; pslot = c3 * 64 + 32 + head; }
    else if (br1) { pact = lane < 8 && cc <= 7; pslot = (cc * 32 + 4 * g1 + (lane & 3)) * 64 + (ti1 - 2 * cc + 2); }
    else { pact = true; pslot = ((lane >> 2) * 32 + 4 * g1 + (lane & 3)) * 64 + 16 + ti1; }
    if (!pact) pslot = cslot;
  }
  u64 cwant = 0, pval = 0;
  Polls polls = {0, 0, 0};

  {
    const u16* x = br1 ? xB : xA;
    const u16* W = br1 ? wB : wA;
    const int tt = w & 3, hp = w >> 2;
    const uint32_t xo0 = (uint32_t)((ti1 * 128 + 16 * w + (lane >> 3)) * DM + (((lane & 7) ^ ((lane >> 4) & 7)) * 8)) * 2u;
    const uint32_t xo1 = (uint32_t)((ti1 * 128 + 16 * w + 8 + (lane >> 3)) * DM + (((lane & 7) ^ ((4 + (lane >> 4)) & 7)) * 8)) * 2u;
    const uint32_t wo0 = (uint32_t)((8 * w + (lane >> 3)) * 1536 + g1 * 64 + (((lane & 7) ^ (((lane >> 4) & 1) << 2)) * 8)) * 2u;
    const int s0 = ((idx & 7) + 2 * (idx >> 3)) & 7;
    auto stage_load = [&](int su, int buf, int part) {
      const int s = (su + s0) & 7;
      const uint32_t b = sm0 + buf * STAGE_BYTES;
      if (part == 0) {
        glds16s(x + s * 64, xo0, b + w * 2048);
        glds16s(x + s * 64, xo1, b + w * 2048 + 1024);
      } else if (part == 1) {
        glds16s(W + (size_t)s * 64 * 1536, wo0, b + 16384 + w * 1024);
        glds16s(W + (size_t)s * 64 * 1536 + 512, wo0, b + 16384 + 8192 + w * 1024);
      } else {
        glds16s(W + (size_t)s * 64 * 1536 + 1024, wo0, b + 16384 + 16384 + w * 1024);
      }
    };
    if (w == 1) glds16s(wphi + (size_t)(head & ~1) * 256, (uint32_t)lane * 16u, sm0 + L_WPHI2);
    if (br1 == 0 && w >= 6) glds16s(wphi + (size_t)(4 * g1) * 256 + (w - 6) * 512, (uint32_t)lane * 16u, sm0 + L_WPHI + (w - 6) * 1024);
    for (int p = 0; p < 3; ++p) stage_load(0, 0, p);
    for (int p = 0; p < 3; ++p) stage_load(1, 1, p);
    cwant = __hip_atomic_load((const gu64*)(ws + WS_E) + cslot, RLX_AGENT);
    pval = __hip_atomic_load((const gu64*)(ws + WS_E) + pslot, RLX_AGENT);

    const int xrow = 32 * tt + (lane & 31);
    const int xa0 = xrow * 128 + ((h ^ ((xrow >> 1) & 7)) * 16);
    const int q4 = (lane & 15) >> 2, p4 = lane & 3;
    const int wa0 = 16384 + (8 * h + q4) * 128 + (((4 * hp + 2 * ((lane >> 4) & 1) + (p4 >> 1)) ^ (((q4 >> 1) & 1) << 2)) * 16) + 8 * (p4 & 1);

    f32x16 aq = zero16(), ak = zero16(), av = zero16();
    bf16x8 xf[2], fq[2], fk[2], fv[2];
    auto frags = [&](const char* b, int ks, int p) {
      xf[p] = lds_read16(b + (xa0 ^ (32 * ks)));
      const char* wb = b + wa0 + ks * 2048;
      fq[p] = tr_frag(wb, wb + 512);
      fk[p] = tr_frag(wb + 8192, wb + 8192 + 512);
      fv[p] = tr_frag(wb + 16384, wb + 16384 + 512);
    };
    auto mma = [&](int p) {
      aq = MFMA32(fq[p], xf[p], aq);
      ak = MFMA32(fk[p], xf[p], ak);
      av = MFMA32(xf[p], fv[p], av);
    };
#pragma unroll
    for (int s = 0; s < 8; ++s) {
      if (s == 0) asm volatile("s_waitcnt vmcnt(7)" ::: "memory");
      else if (s < 7) asm volatile("s_waitcnt vmcnt(5)" ::: "memory");
      else asm volatile("s_waitcnt vmcnt(0)" ::: "memory");
      __builtin_amdgcn_s_barrier();
      asm volatile("" ::: "memory");
      if (s == 0) STAMP(1);
      const char* b = smem + (s % 3) * STAGE_BYTES;
      frags(b, 0, 0);
      if (s + 2 < 8) stage_load(s + 2, (s + 2) % 3, 0);
      __builtin_amdgcn_sched_barrier(0);
      if (s > 0) mma(1);
      __builtin_amdgcn_sched_barrier(0);
      frags(b, 1, 1);
      if (s + 2 < 8) stage_load(s + 2, (s + 2) % 3, 1);
      __builtin_amdgcn_sched_barrier(0);
      mma(0);
      __builtin_amdgcn_sched_barrier(0);
      frags(b, 2, 0);
      if (s + 2 < 8) stage_load(s + 2, (s + 2) % 3, 2);
      __builtin_amdgcn_sched_barrier(0);
      mma(1);
      __builtin_amdgcn_sched_barrier(0);
      frags(b, 3, 1);
      __builtin_amdgcn_sched_barrier(0);
      mma(0);
      __builtin_amdgcn_sched_barrier(0);
      asm volatile("s_waitcnt lgkmcnt(0)" ::: "memory");
    }
    mma(1);
    STAMP(2);
    const int tile32 = ti1 * 4 + tt, head0 = 4 * g1 + 2 * hp, blk = (lane >> 4) & 1;
    if (br1) {
      const uint32_t qo = (head0 * NT + tile32) * 1024 + lane * 16;
      st_sc1_frag(wsr, WS_QB + qo, pack8s<0>(aq, 0.25f * L2E));
      st_sc1_frag(wsr, WS_QB + qo + NT * 1024, pack8s<8>(aq, 0.25f * L2E));
      st_sc1_frag(wsr, WS_KB + qo, pack8<0>(ak));
      st_sc1_frag(wsr, WS_KB + qo + NT * 1024, pack8<8>(ak));
      const uint32_t vo = WS_VB + ((head0 + ((lane >> 4) & 1)) * NT + tile32) * 1024 + h * 256 + (lane & 15) * 16;
      st_sc1_frag(wsr, vo, pack8<0>(av));
      st_sc1_frag(wsr, vo + 512, pack8<8>(av));
    } else {
      const uint32_t qo = WS_QA + (head0 * NT + tile32) * 1024 + lane * 16;
      st_sc1_frag(wsr, qo, pack8<0>(aq));
      st_sc1_frag(wsr, qo + NT * 1024, pack8<8>(aq));
      s16x8 i0, i1;
#pragma unroll
      for (int j = 0; j < 8; ++j) {
        const int f = 8 * (j >> 2) + 4 * h + (j & 3);
        i0[j] = ((lane & 31) == f) ? (short)0x3F80 : (short)0;
        i1[j] = ((lane & 31) == 16 + f) ? (short)0x3F80 : (short)0;
      }
      const bf16x8 id0 = __builtin_bit_cast(bf16x8, i0), id1 = __builtin_bit_cast(bf16x8, i1);
      const u32x4 ones = {0x3F803F80u, 0x3F803F80u, 0x3F803F80u, 0x3F803F80u};
      const u32x4 vf0 = __builtin_bit_cast(u32x4, pack8<0>(av)), vf1 = __builtin_bit_cast(u32x4, pack8<8>(av));
      f32x16 pk0 = MFMA32(wphi_frag_lds(smem + L_WPHI + (2 * hp) * 512, lane), pack8<0>(ak), zero16());
      f32x16 pk1 = MFMA32(wphi_frag_lds(smem + L_WPHI + (2 * hp + 1) * 512, lane), pack8<8>(ak), zero16());
      STAMP(13);
      bf16x8 fp[2], fn[2];
      dual_softmax(pk0, fp[0], fn[0]);
      dual_softmax(pk1, fp[1], fn[1]);
      STAMP(14);
#pragma unroll
      for (int p = 0; p < 2; ++p) {
        f32x16 T = MFMA32(fp[p], id0, zero16());
        T = MFMA32(fn[p], id1, T);
        const bool mine = blk == p;
        f32x16 kvp = MFMA32(pack8<0>(T), __builtin_bit_cast(bf16x8, mine ? vf0 : ones), zero16());
        kvp = MFMA32(pack8<8>(T), __builtin_bit_cast(bf16x8, mine ? vf1 : ones), kvp);
        if (mine || (lane & 15) == 0) {
          char* dst = smem + ((2 * hp + p) * 4 + tt) * 2176 + (mine ? (h * 16 + (lane & 15)) * 16 : 2048 + h * 16);
          const int st = mine ? 512 : 32;
#pragma unroll
          for (int r4 = 0; r4 < 4; ++r4) {
            f32x4 v = {kvp[4 * r4], kvp[4 * r4 + 1], kvp[4 * r4 + 2], kvp[4 * r4 + 3]};
            *(f32x4*)(dst + r4 * st) = v;
          }
        }
      }
      wg_barrier();
      STAMP(15);
      if (w < 4 || (w == 4 && lane < 16)) {
        const int hh = w < 4 ? w : (lane >> 2);
        const char* src = smem + hh * 4 * 2176 + (w < 4 ? (lane >> 5) * 1024 + (lane & 31) * 16 : 2048 + (lane & 3) * 32);
        const int d2 = w < 4 ? 512 : 16;
        f32x4 s0v = *(const f32x4*)src, s1v = *(const f32x4*)(src + d2);
#pragma unroll
        for (int t = 1; t < 4; ++t) {
          s0v += *(const f32x4*)(src + t * 2176);
          s1v += *(const f32x4*)(src + t * 2176 + d2);
        }
        bf16x8 o;
#pragma unroll
        for (int c = 0; c < 4; ++c) { o[c] = (__bf16)s0v[c]; o[4 + c] = (__bf16)s1v[c]; }
        st_sc1_frag(wsr, WS_KVP + ((4 * g1 + hh) * 16 + ti1) * KVP_BYTES + (w < 4 ? lane * 16 : 1024 + (lane & 3) * 16), o);
      }
    }
    u64 first = 0;
    if (w == 0) first = poll_slot(ws, bid * 64 + (lane & 3));
    drain_barrier();
    STAMP(3);
    cwant = mix(cwant);
    if (w == 0) {
      pval = mix(pval);
      signal_slots(ws, pslot, pval, pact && lane < 32);
      wait_slots(ws, bid * 64 + (lane & 3), cwant, lane < 4 && 2 * chunk - 2 + lane >= 0, first, polls);
    }
    if (tid >= 448) {
      const u32x4 ones = {0x3F803F80u, 0x3F803F80u, 0x3F803F80u, 0x3F803F80u};
      *(u32x4*)(smem + L_ONES + (tid - 448) * 16) = ones;
    }
    wg_barrier();
    STAMP(4);
  }

  const int qt = chunk * 8 + w;
  const bool lower = (lane & 16) == 0;
  bf16x8 qf;
  {
    const int t0 = chunk * 8 - 8;
    u32x4 kv[4];
    if (t0 + 2 * w >= 0) {
      const uint32_t o = (head * NT + t0 + 2 * w) * 1024 + lane * 16;
      kv[0] = ld_sc1(wsr, WS_KB + o);
      kv[1] = ld_sc1(wsr, WS_KB + o + 1024);
      kv[2] = ld_sc1(wsr, WS_VB + o);
      kv[3] = ld_sc1(wsr, WS_VB + o + 1024);
    }
    qf = ld_sc1_frag(wsr, WS_QB + (head * NT + qt) * 1024 + lane * 16);
    if (t0 + 2 * w >= 0) {
      *(u32x4*)(smem + L_K + 2 * w * 1024 + lane * 16) = kv[0];
      *(u32x4*)(smem + L_K + 2 * w * 1024 + 1024 + lane * 16) = kv[1];
      *(u32x4*)(smem + L_V + 2 * w * 1024 + lane * 16) = kv[2];
      *(u32x4*)(smem + L_V + 2 * w * 1024 + 1024 + lane * 16) = kv[3];
    }
    poll_retire(polls);
    wg_barrier();
    STAMP(5);
  }
  const bool aslot = lane >= 16 && lane < 32;
  const u64 early = __hip_atomic_load((const gu64*)(ws + WS_F) + cslot, RLX_AGENT);
  u32x4 pp[2], pz;
  bf16x8 qa;
  const u16* wo = br3 ? woB : woA;
  auto wo_load = [&]() {
    const uint32_t off = (uint32_t)((64 * w + (lane >> 3)) * DM + nj * 64 + (((lane & 7) ^ (((lane >> 4) & 1) << 2)) * 8)) * 2u;
#pragma unroll
    for (int i = 0; i < 8; ++i) glds16s(wo + i * 8 * DM, off, sm0 + L_WO + w * 8192 + i * 1024);
  };

  {
    const char* kbase = smem + L_K + w * 1024 + lane * 16;
    const int vinc = lower ? 1024 : 0;
    const char* vbase = lower ? (smem + L_V + w * 1024 + h * 256 + (lane & 15) * 16) : (smem + L_ONES + (lane & 15) * 16);
    const int ql = lane & 31;
    const int first = chunk > 0 ? 0 : 8 - w;
    auto a_fetch = [&]() {
      if (__builtin_amdgcn_ballot_w64(aslot && early != cwant) != 0) spin_slots(ws, cslot, cwant, aslot);
      asm volatile("" ::: "memory");
      if (w == 0 && aslot) __hip_atomic_store((gu64*)(ws + WS_E) + cslot, cwant, RLX_AGENT);
      const uint32_t kb = WS_KVP + (head * 16 + 2 * w) * KVP_BYTES;
      pp[0] = ld_sc1(wsr, kb + lane * 16);
      pp[1] = ld_sc1(wsr, kb + KVP_BYTES + lane * 16);
      pz = ld_sc1(wsr, kb + ((lane >> 2) & 1) * KVP_BYTES + 1024 + (lane & 3) * 16);
      qa = ld_sc1_frag(wsr, WS_QA + (head * NT + qt) * 1024 + lane * 16);
    };
    auto a_readout = [&]() {
      f32x4 s0v, s1v, z0, z1;
      s0v[0] = __uint_as_float(pp[0][0] << 16) + __uint_as_float(pp[1][0] << 16);
      s0v[1] = __uint_as_float(pp[0][0] & 0xFFFF0000u) + __uint_as_float(pp[1][0] & 0xFFFF0000u);
      s0v[2] = __uint_as_float(pp[0][1] << 16) + __uint_as_float(pp[1][1] << 16);
      s0v[3] = __uint_as_float(pp[0][1] & 0xFFFF0000u) + __uint_as_float(pp[1][1] & 0xFFFF0000u);
      s1v[0] = __uint_as_float(pp[0][2] << 16) + __uint_as_float(pp[1][2] << 16);
      s1v[1] = __uint_as_float(pp[0][2] & 0xFFFF0000u) + __uint_as_float(pp[1][2] & 0xFFFF0000u);
      s1v[2] = __uint_as_float(pp[0][3] << 16) + __uint_as_float(pp[1][3] << 16);
      s1v[3] = __uint_as_float(pp[0][3] & 0xFFFF0000u) + __uint_as_float(pp[1][3] & 0xFFFF0000u);
      z0[0] = __uint_as_float(pz[0] << 16); z0[1] = __uint_as_float(pz[0] & 0xFFFF0000u);
      z0[2] = __uint_as_float(pz[1] << 16); z0[3] = __uint_as_float(pz[1] & 0xFFFF0000u);
      z1[0] = __uint_as_float(pz[2] << 16); z1[1] = __uint_as_float(pz[2] & 0xFFFF0000u);
      z1[2] = __uint_as_float(pz[3] << 16); z1[3] = __uint_as_float(pz[3] & 0xFFFF0000u);
#pragma unroll
      for (int c = 0; c < 4; ++c) { z0[c] += __shfl_xor(z0[c], 4); z1[c] += __shfl_xor(z1[c], 4); }
      char* red = smem + L_RED;
      *(f32x4*)(red + w * 2048 + lane * 16) = s0v;
      *(f32x4*)(red + w * 2048 + 1024 + lane * 16) = s1v;
      if (lane < 4) {
        *(f32x4*)(red + 16384 + w * 128 + lane * 32) = z0;
        *(f32x4*)(red + 16384 + w * 128 + lane * 32 + 16) = z1;
      }
      wo_load();
      const bf16x8 wf = wphi_frag_lds(smem + L_WPHI2 + (head & 1) * 512, lane);
      const f32x16 pq = MFMA32(wf, qa, zero16());
      wg_barrier();
      STAMP(7);
      if (w == 0) {
        const char* src = red + lane * 16;
        f32x4 t0 = *(const f32x4*)src, t1 = *(const f32x4*)(src + 1024);
#pragma unroll
        for (int t = 1; t < 8; ++t) { t0 += *(const f32x4*)(src + t * 2048); t1 += *(const f32x4*)(src + t * 2048 + 1024); }
        bf16x8 o;
#pragma unroll
        for (int c = 0; c < 4; ++c) { o[c] = (__bf16)t0[c]; o[4 + c] = (__bf16)t1[c]; }
        *(bf16x8*)(smem + L_KVF + (lane >> 5) * 1024 + (((lane >> 4) & 1) * 32 + (lane & 15)) * 16) = o;
      }
      if (w == 1) {
        const int f = lane & 31;
        const float* zr = (const float*)(red + 16384);
        const __bf16 z = (__bf16)(((zr[f] + zr[32 + f]) + (zr[64 + f] + zr[96 + f])) + ((zr[128 + f] + zr[160 + f]) + (zr[192 + f] + zr[224 + f])));
#pragma unroll
        for (int i = 0; i < 8; ++i)
          *(__bf16*)(smem + L_KVF + (f >> 4) * 1024 + (((f >> 2) & 1) * 32 + 16 + 8 * h + i) * 16 + (((f >> 3) & 1) * 4 + (f & 3)) * 2) = z;
      }
      bf16x8 fp, fn;
      dual_softmax(pq, fp, fn);
      wg_barrier();
      f32x16 oa = MFMA32(lds_read16(smem + L_KVF + lane * 16), fp, zero16());
      oa = MFMA32(lds_read16(smem + L_KVF + 1024 + lane * 16), fn, oa);
      const float inv = __builtin_amdgcn_rcpf(oa[8] + 1e-6f);
      st_sc1_frag(wsr, WS_O + (head * NT + qt) * 1024 + lane * 16, pack8s<0>(oa, inv));
      STAMP(8);
    };
    auto boundary_p = [&](const f32x16& sm, float m, u32x4& da, u32x4& db, u32x4& ua, u32x4& ub) {
      f32x16 e;
#pragma unroll
      for (int r = 0; r < 16; ++r) e[r] = __builtin_amdgcn_exp2f(sm[r] - m);
      const u32x4 pa = __builtin_bit_cast(u32x4, pack8<0>(e)), pb = __builtin_bit_cast(u32x4, pack8<8>(e));
#pragma unroll
      for (int i = 0; i < 4; ++i) {
        const int ra = 2 * i, rb = 8 + 2 * i;
        const int ka = (ra & 3) + 8 * (ra >> 2) + 4 * h, kb = (rb & 3) + 8 * (rb >> 2) + 4 * h;
        const unsigned ma = (ka <= ql ? 0xFFFFu : 0u) | (ka + 1 <= ql ? 0xFFFF0000u : 0u);
        const unsigned mb = (kb <= ql ? 0xFFFFu : 0u) | (kb + 1 <= ql ? 0xFFFF0000u : 0u);
        da[i] = pa[i] & ma; ua[i] = pa[i] & ~ma;
        db[i] = pb[i] & mb; ub[i] = pb[i] & ~mb;
      }
    };
    f32x16 o = zero16();
    if (chunk > 0) {
      bf16x8 kf[9];
      kf[8] = lds_read16(kbase + 8 * 1024);
      kf[0] = lds_read16(kbase);
      kf[1] = lds_read16(kbase + 1024);
      kf[2] = lds_read16(kbase + 2048);
      f32x16 sm = MFMA32(kf[8], qf, zero16());
      f32x16 s0 = MFMA32(kf[0], qf, zero16());
      f32x16 sc = MFMA32(kf[1], qf, zero16());
#pragma unroll
      for (int r = 0; r < 16; ++r) {
        const int kl = (r & 3) + 8 * (r >> 2) + 4 * h;
        sm[r] = (kl <= ql) ? sm[r] : s0[r];
      }
      float m = sm[0];
#pragma unroll
      for (int r = 1; r < 16; ++r) m = fmaxf(m, sm[r]);
#pragma unroll
      for (int i = 1; i < 8; ++i) {
        f32x16 sn;
        if (i + 2 < 8) kf[i + 2] = lds_read16(kbase + (i + 2) * 1024);
        if (i < 7) sn = MFMA32(kf[i + 1], qf, zero16());
        __builtin_amdgcn_sched_barrier(0);
#pragma unroll
        for (int r = 0; r < 16; r += 2) m = fmaxf(fmaxf(m, sc[r]), sc[r + 1]);
        __builtin_amdgcn_sched_barrier(0);
        if (i < 7) sc = sn;
      }
      m = swap_max(m);
      a_fetch();
      f32x16 negm;
#pragma unroll
      for (int r = 0; r < 16; ++r) negm[r] = -m;
      sc = MFMA32(kf[1], qf, negm);
      {
        const bf16x8 vd0 = lds_read16(vbase + 8 * vinc), vd1 = lds_read16(vbase + 8 * vinc + 512);
        const bf16x8 vu0 = lds_read16(vbase), vu1 = lds_read16(vbase + 512);
        u32x4 da, db, ua, ub;
        boundary_p(sm, m, da, db, ua, ub);
        o = MFMA32(vd0, __builtin_bit_cast(bf16x8, da), o);
        o = MFMA32(vd1, __builtin_bit_cast(bf16x8, db), o);
        o = MFMA32(vu0, __builtin_bit_cast(bf16x8, ua), o);
        o = MFMA32(vu1, __builtin_bit_cast(bf16x8, ub), o);
      }
#pragma unroll
      for (int i = 1; i < 8; ++i) {
        const bf16x8 v0 = lds_read16(vbase + i * vinc), v1 = lds_read16(vbase + i * vinc + 512);
        f32x16 sn, e;
        __builtin_amdgcn_sched_barrier(0);
#pragma unroll
        for (int r = 0; r < 4; ++r) e[r] = __builtin_amdgcn_exp2f(sc[r]);
        __builtin_amdgcn_sched_barrier(0);
        if (i < 7) sn = MFMA32(kf[i + 1], qf, negm);
        __builtin_amdgcn_sched_barrier(0);
#pragma unroll
        for (int r = 4; r < 8; ++r) e[r] = __builtin_amdgcn_exp2f(sc[r]);
        const bf16x8 p0 = pack8<0>(e);
#pragma unroll
        for (int r = 8; r < 10; ++r) e[r] = __builtin_amdgcn_exp2f(sc[r]);
        __builtin_amdgcn_sched_barrier(0);
        o = MFMA32(v0, p0, o);
        __builtin_amdgcn_sched_barrier(0);
#pragma unroll
        for (int r = 10; r < 16; ++r) e[r] = __builtin_amdgcn_exp2f(sc[r]);
        const bf16x8 p1 = pack8<8>(e);
        __builtin_amdgcn_sched_barrier(0);
        o = MFMA32(v1, p1, o);
        __builtin_amdgcn_sched_barrier(0);
        if (i < 7) sc = sn;
        if (i == A_AT) { a_readout(); __builtin_amdgcn_sched_barrier(0); }
      }
    } else {
      f32x16 sm = MFMA32(lds_read16(kbase + 8 * 1024), qf, zero16());
#pragma unroll
      for (int r = 0; r < 16; ++r) {
        const int kl = (r & 3) + 8 * (r >> 2) + 4 * h;
        sm[r] = (kl <= ql) ? sm[r] : -INFINITY;
      }
      float m = sm[0];
#pragma unroll
      for (int r = 1; r < 16; ++r) m = fmaxf(m, sm[r]);
#pragma unroll 1
      for (int i = first; i < 8; ++i) {
        const f32x16 s = MFMA32(lds_read16(kbase + i * 1024), qf, zero16());
#pragma unroll
        for (int r = 0; r < 16; r += 2) m = fmaxf(fmaxf(m, s[r]), s[r + 1]);
      }
      m = swap_max(m);
      a_fetch();
      u32x4 da, db, ua, ub;
      boundary_p(sm, m, da, db, ua, ub);
      o = MFMA32(lds_read16(vbase + 8 * vinc), __builtin_bit_cast(bf16x8, da), o);
      o = MFMA32(lds_read16(vbase + 8 * vinc + 512), __builtin_bit_cast(bf16x8, db), o);
#pragma unroll 1
      for (int i = first; i < 8; ++i) {
        const f32x16 s = MFMA32(lds_read16(kbase + i * 1024), qf, zero16());
        f32x16 e;
#pragma unroll
        for (int r = 0; r < 16; ++r) e[r] = __builtin_amdgcn_exp2f(s[r] - m);
        o = MFMA32(lds_read16(vbase + i * vinc), pack8<0>(e), o);
        o = MFMA32(lds_read16(vbase + i * vinc + 512), pack8<8>(e), o);
      }
      a_readout();
    }
    const float inv = __builtin_amdgcn_rcpf(o[8]);
    st_sc1_frag(wsr, WS_O + FRAG_BYTES + (head * NT + qt) * 1024 + lane * 16, pack8s<0>(o, inv));
    STAMP(6);
    STAMPW(16);
    drain_barrier();
    STAMP(9);
    if (w == 0) {
      signal_slots(ws, pslot, pval, lane >= 32);
      wait_slots(ws, cslot | 32, cwant, lane >= 32, ~cwant, polls);
    }
    wg_barrier();
    STAMP(10);
  }

  {
    const int tt = w & 3, kh = w >> 2;
    const int tile32 = ti3 * 4 + tt;
    const uint32_t og = WS_O + br3 * FRAG_BYTES + ((16 * kh) * NT + tile32) * 1024 + lane * 16;
    bf16x8 of[16];
#pragma unroll
    for (int i = 0; i < 16; ++i) of[i] = ld_sc1_frag(wsr, og + i * NT * 1024);
    __builtin_amdgcn_sched_barrier(0);
    const int blk = (lane >> 4) & 1, q4 = (lane & 15) >> 2, p4 = lane & 3;
    f32x16 acc0 = zero16(), acc1 = zero16();
    const char* wl = smem + L_WO;
    const int ra0 = (256 * kh + 4 * h + q4) * 128 + (((2 * blk + (p4 >> 1)) ^ (((q4 >> 1) & 1) << 2)) * 16) + 8 * (p4 & 1);
    const int ra1 = (256 * kh + 4 * h + q4) * 128 + (((4 + 2 * blk + (p4 >> 1)) ^ (((q4 >> 1) & 1) << 2)) * 16) + 8 * (p4 & 1);
#pragma unroll
    for (int i = 0; i < 16; ++i) {
      const int kb = 16 * i * 128;
      const bf16x8 a0 = tr_frag(wl + ra0 + kb, wl + ra0 + kb + 1024);
      const bf16x8 a1 = tr_frag(wl + ra1 + kb, wl + ra1 + kb + 1024);
      acc0 = MFMA32(a0, of[i], acc0);
      acc1 = MFMA32(a1, of[i], acc1);
    }
    poll_retire(polls);
    STAMP(11);
    float* ex = (float*)smem + w * 1024 + lane * 4;
    const f32x16 give = kh ? acc0 : acc1;
    f32x16 keep = kh ? acc1 : acc0;
#pragma unroll
    for (int r4 = 0; r4 < 4; ++r4) {
      f32x4 v = {give[4 * r4], give[4 * r4 + 1], give[4 * r4 + 2], give[4 * r4 + 3]};
      *(f32x4*)(ex + r4 * 256) = v;
    }
    wg_barrier();
    const float* px = (const float*)smem + (w ^ 4) * 1024 + lane * 4;
#pragma unroll
    for (int r4 = 0; r4 < 4; ++r4) {
      const f32x4 v = *(const f32x4*)(px + r4 * 256);
#pragma unroll
      for (int c = 0; c < 4; ++c) keep[4 * r4 + c] += v[c];
    }
    const u32x4 lo = __builtin_bit_cast(u32x4, pack8<0>(keep)), hi = __builtin_bit_cast(u32x4, pack8<8>(keep));
    u16* op = out + (size_t)(tile32 * 32 + (lane & 31)) * 1024 + br3 * 512 + nj * 64 + kh * 32 + h * 8;
    auto t0 = __builtin_amdgcn_permlane32_swap(lo[0], lo[2], false, false);
    auto t1 = __builtin_amdgcn_permlane32_swap(lo[1], lo[3], false, false);
    u32x4 v = {t0[0], t1[0], t0[1], t1[1]};
    OUT_ST(op, v);
    auto t2 = __builtin_amdgcn_permlane32_swap(hi[0], hi[2], false, false);
    auto t3 = __builtin_amdgcn_permlane32_swap(hi[1], hi[3], false, false);
    u32x4 u = {t2[0], t3[0], t2[1], t3[1]};
    OUT_ST(op + 16, u);
  }
  STAMP(12);
}

extern "C" void kernel_launch(void* const* d_in, const int* in_sizes, int n_in, void* d_out, int out_size,
                              void* d_ws, size_t ws_size, hipStream_t stream) {
  fused<<<256, 512, 0, stream>>>((const u16*)d_in[0], (const u16*)d_in[1], (const u16*)d_in[2], (const u16*)d_in[3],
                                 (const u16*)d_in[4], (const u16*)d_in[5], (const u16*)d_in[6], (char*)d_ws, (u16*)d_out);
}
```

```cpp
#include <hip/hip_runtime.h>
#include <hip/hip_bf16.h>
#include <stdint.h>

typedef __bf16 bf16x8 __attribute__((ext_vector_type(8)));
typedef __bf16 bf16x4 __attribute__((ext_vector_type(4)));
typedef short s16x4 __attribute__((ext_vector_type(4)));
typedef short s16x8 __attribute__((ext_vector_type(8)));
typedef float f32x16 __attribute__((ext_vector_type(16)));
typedef float f32x4 __attribute__((ext_vector_type(4)));
typedef unsigned u32x4 __attribute__((ext_vector_type(4)));
typedef unsigned u32x2 __attribute__((ext_vector_type(2)));
typedef unsigned short u16;

#define LDS_AS __attribute__((address_space(3)))
#define GLB_AS __attribute__((address_space(1)))
#define DEVI __device__ __forceinline__
#define MFMA32(a, b, c) __builtin_amdgcn_mfma_f32_32x32x16_bf16((a), (b), (c), 0, 0, 0)

#define STAMPW(i)
#define STAMP(i)
constexpr int SEQ = 2048, DM = 512, NH = 32, NT = 64;
constexpr float L2E = 1.4426950408889634f;

constexpr uint32_t FRAG_BYTES = NH * NT * 1024;
constexpr uint32_t WS_QA = 0, WS_QB = FRAG_BYTES, WS_KB = 2 * FRAG_BYTES, WS_VB = 3 * FRAG_BYTES;
constexpr uint32_t WS_O = 4 * FRAG_BYTES;
constexpr uint32_t WS_KVP = 6 * FRAG_BYTES;
constexpr uint32_t KVP_BYTES = 1088;
constexpr uint32_t WS_E = WS_KVP + NH * 16 * KVP_BYTES;
constexpr uint32_t WS_F = WS_E + 256 * 64 * 8;
constexpr uint32_t WS_END = WS_F + 256 * 64 * 8;

DEVI void glds16s(const void* sbase, uint32_t voff, uint32_t lds_addr) {
  asm volatile("s_mov_b32 m0, %2\n\ts_nop 0\n\tglobal_load_lds_dwordx4 %0, %1" ::"v"(voff), "s"(sbase), "s"(lds_addr) : "memory");
}
DEVI void glds4s(const void* sbase, uint32_t voff, uint32_t lds_addr) {
  asm volatile("s_mov_b32 m0, %2\n\ts_nop 0\n\tglobal_load_lds_dword %0, %1" ::"v"(voff), "s"(sbase), "s"(lds_addr) : "memory");
}
DEVI uint32_t lds_addr_of(const char* p) { return (uint32_t)(uintptr_t)(const LDS_AS char*)p; }
DEVI bf16x8 lds_read16(const char* p) { return *(const bf16x8*)p; }
DEVI bf16x8 tr_frag(const char* p0, const char* p1) {
  s16x4 a = __builtin_amdgcn_ds_read_tr16_b64_v4i16((LDS_AS s16x4*)p0);
  s16x4 b = __builtin_amdgcn_ds_read_tr16_b64_v4i16((LDS_AS s16x4*)p1);
  s16x8 r = __builtin_shufflevector(a, b, 0, 1, 2, 3, 4, 5, 6, 7);
  return __builtin_bit_cast(bf16x8, r);
}
DEVI float max2(float a, float b) { return __builtin_elementwise_maximum(a, b); }
DEVI float max3(float a, float b, float c) { return max2(max2(a, b), c); }
DEVI float swap_max(float x) {
  auto t = __builtin_amdgcn_permlane32_swap(__float_as_uint(x), __float_as_uint(x), false, false);
  return max2(__uint_as_float(t[0]), __uint_as_float(t[1]));
}
DEVI float swap_add(float x) {
  auto t = __builtin_amdgcn_permlane32_swap(__float_as_uint(x), __float_as_uint(x), false, false);
  return __uint_as_float(t[0]) + __uint_as_float(t[1]);
}
typedef float f32x2 __attribute__((ext_vector_type(2)));
typedef __bf16 bf16x2 __attribute__((ext_vector_type(2)));
DEVI unsigned cvt2(float a, float b) {
  const f32x2 v = {a, b};
  return __builtin_bit_cast(unsigned, __builtin_convertvector(v, bf16x2));
}
template <int O>
DEVI bf16x8 pack8(const f32x16& a) {
  const u32x4 r = {cvt2(a[O], a[O + 1]), cvt2(a[O + 2], a[O + 3]), cvt2(a[O + 4], a[O + 5]), cvt2(a[O + 6], a[O + 7])};
  return __builtin_bit_cast(bf16x8, r);
}
template <int O>
DEVI bf16x8 pack8s(const f32x16& a, float s) {
  const u32x4 r = {cvt2(a[O] * s, a[O + 1] * s), cvt2(a[O + 2] * s, a[O + 3] * s), cvt2(a[O + 4] * s, a[O + 5] * s),
                   cvt2(a[O + 6] * s, a[O + 7] * s)};
  return __builtin_bit_cast(bf16x8, r);
}
DEVI f32x16 zero16() {
  f32x16 z;
#pragma unroll
  for (int i = 0; i < 16; ++i) z[i] = 0.f;
  return z;
}
DEVI bf16x8 wphi_frag_lds(const char* wl, int lane) {
  const char* p = wl + (4 * (lane >> 5) + ((lane & 15) >> 2)) * 32 + (lane & 3) * 8;
  const u32x4 r = __builtin_bit_cast(u32x4, tr_frag(p, p + 256));
  const unsigned sgn = (lane & 16) ? 0x80008000u : 0u;
  const u32x4 o = {r[0] ^ sgn, r[1] ^ sgn, r[2] ^ sgn, r[3] ^ sgn};
  return __builtin_bit_cast(bf16x8, o);
}
DEVI void dual_softmax(const f32x16& p, bf16x8& fp, bf16x8& fn) {
  float mp = max3(max3(p[0], p[1], p[2]), max3(p[3], p[4], p[5]), max2(p[6], p[7]));
  float mn = max3(max3(p[8], p[9], p[10]), max3(p[11], p[12], p[13]), max2(p[14], p[15]));
  mp = swap_max(mp) * L2E;
  mn = swap_max(mn) * L2E;
  f32x16 e;
  float sp = 0.f, sn = 0.f;
#pragma unroll
  for (int j = 0; j < 8; ++j) {
    e[j] = __builtin_amdgcn_exp2f(__builtin_fmaf(p[j], L2E, -mp));
    e[8 + j] = __builtin_amdgcn_exp2f(__builtin_fmaf(p[8 + j], L2E, -mn));
    sp += e[j];
    sn += e[8 + j];
  }
  sp = __builtin_amdgcn_rcpf(swap_add(sp));
  sn = __builtin_amdgcn_rcpf(swap_add(sn));
  fp = pack8s<0>(e, sp);
  fn = pack8s<8>(e, sn);
}

typedef unsigned long long u64;
typedef GLB_AS u64 gu64;
#define RLX_AGENT __ATOMIC_RELAXED, __HIP_MEMORY_SCOPE_AGENT
constexpr int AUX_SC1 = 16;
DEVI u32x4 ld_sc1(__amdgpu_buffer_rsrc_t r, uint32_t off) { return __builtin_amdgcn_raw_buffer_load_b128(r, off, 0, AUX_SC1); }
DEVI bf16x8 ld_sc1_frag(__amdgpu_buffer_rsrc_t r, uint32_t off) { return __builtin_bit_cast(bf16x8, ld_sc1(r, off)); }
DEVI void st_sc1(__amdgpu_buffer_rsrc_t r, uint32_t off, u32x4 v) { __builtin_amdgcn_raw_buffer_store_b128(v, r, off, 0, AUX_SC1); }
DEVI void st_sc1_frag(__amdgpu_buffer_rsrc_t r, uint32_t off, bf16x8 v) { st_sc1(r, off, __builtin_bit_cast(u32x4, v)); }
DEVI void wg_barrier() {
  asm volatile("s_waitcnt lgkmcnt(0)" ::: "memory");
  __builtin_amdgcn_s_barrier();
  asm volatile("" ::: "memory");
}
DEVI void drain_barrier() {
  asm volatile("s_waitcnt vmcnt(0) lgkmcnt(0)" ::: "memory");
  __builtin_amdgcn_s_barrier();
  asm volatile("" ::: "memory");
}
#define POLL_GAP 8
DEVI u64 mix(u64 e) { return e * 0x9E3779B97F4A7C15ull + 0x632BE59BD9B4E019ull; }
struct Polls { u64 a, b, c; };
DEVI u64 poll_slot(char* ws, uint32_t poll) { return __hip_atomic_load((const gu64*)(ws + WS_F) + poll, RLX_AGENT); }
#define POLL_ISSUE(r) asm volatile("global_load_dwordx2 %0, %1, %2 sc1" : "=v"(r) : "v"(off), "s"(base) : "memory")
#define POLL_WAIT(r) asm volatile("s_waitcnt vmcnt(2)" : "+v"(r)::"memory")
DEVI void wait_slots(char* ws, uint32_t poll, u64 want, bool active, u64 first, Polls& p) {
  if (__builtin_amdgcn_ballot_w64(active && first != want) != 0) {
    const char* base = ws + WS_F;
    const uint32_t off = poll * 8u;
    POLL_ISSUE(p.a);
    __builtin_amdgcn_s_sleep(POLL_GAP);
    POLL_ISSUE(p.b);
    __builtin_amdgcn_s_sleep(POLL_GAP);
    POLL_ISSUE(p.c);
#pragma unroll 1
    for (int it = 0; it < 100000; ++it) {
      POLL_WAIT(p.a);
      if (__builtin_amdgcn_ballot_w64(active && p.a != want) == 0) break;
      POLL_ISSUE(p.a);
      POLL_WAIT(p.b);
      if (__builtin_amdgcn_ballot_w64(active && p.b != want) == 0) break;
      POLL_ISSUE(p.b);
      POLL_WAIT(p.c);
      if (__builtin_amdgcn_ballot_w64(active && p.c != want) == 0) break;
      POLL_ISSUE(p.c);
    }
  }
  if (active) __hip_atomic_store((gu64*)(ws + WS_E) + poll, want, RLX_AGENT);
}
DEVI void poll_retire(Polls& p) { asm volatile("s_waitcnt vmcnt(0)" : "+v"(p.a), "+v"(p.b), "+v"(p.c)::"memory"); }
DEVI void spin_slots(char* ws, uint32_t slot, u64 want, bool active) {
  const gu64* f = (const gu64*)(ws + WS_F) + slot;
#pragma unroll 1
  for (int it = 0; it < 200000; ++it) {
    const u64 v = active ? __hip_atomic_load(f, RLX_AGENT) : want;
    if (__builtin_amdgcn_ballot_w64(v != want) == 0) break;
    __builtin_amdgcn_s_sleep(1);
  }
}
DEVI void signal_slots(char* ws, uint32_t slot, u64 val, bool active) {
  if (active) __hip_atomic_store((gu64*)(ws + WS_F) + slot, val, RLX_AGENT);
}

#define OUTPOL 0
#define OUT_ST(p, v) (*(u32x4*)(p) = (v))
constexpr uint32_t CODE_PF = 20992;
constexpr int STAGE_BYTES = 40960;
constexpr int L_WPHI = 3 * STAGE_BYTES;
constexpr int L_K = 0, L_V = 16384, L_ONES = 32768, L_RED = 33792, L_KVF = L_RED + 17408, L_WO = 57344;
#define A_AT 3
constexpr int L_WPHI2 = L_WPHI + 2048;
constexpr int L_PF = L_WPHI2 + 1024;
constexpr int LDS_BYTES = L_PF + 256;

__global__ __launch_bounds__(512, 2) void fused(const u16* __restrict__ xA, const u16* __restrict__ wA,
                                                const u16* __restrict__ wphi, const u16* __restrict__ woA,
                                                const u16* __restrict__ xB, const u16* __restrict__ wB,
                                                const u16* __restrict__ woB, char* __restrict__ ws,
                                                u16* __restrict__ out) {
  __shared__ __attribute__((aligned(16))) char smem[LDS_BYTES];
  {
    const char* pc;
    asm volatile("s_getpc_b64 %0" : "=s"(pc));
    const uint32_t o = threadIdx.x * 64u;
    glds4s(pc, o < CODE_PF ? o : 0u, lds_addr_of(smem) + L_PF);
  }
  const int tid = threadIdx.x, lane = tid & 63;
  const int w = __builtin_amdgcn_readfirstlane(tid >> 6);
  const int bid = blockIdx.x, xcd = bid & 7, idx = bid >> 3;
  const int h = lane >> 5;
  const int br1 = xcd >> 2, jx = xcd & 3;
  const int ti1 = (jx & 1) * 8 + (idx & 7);
  const int g1 = (jx >> 1) * 4 + (idx >> 3);
  const int head = bid & 31, chunk = bid >> 5;
  const int br3 = xcd >> 2, ti3 = (xcd & 3) * 4 + (idx & 3), nj = idx >> 2;
  const __amdgpu_buffer_rsrc_t wsr = __builtin_amdgcn_make_buffer_rsrc(ws, 0, WS_END, 0x00020000);
  const uint32_t sm0 = lds_addr_of(smem);

  STAMP(0);
  const uint32_t cslot = bid * 64 + lane;
  uint32_t pslot;
  bool pact;
  u64 cwant = 0, pval = 0;
  Polls polls = {0, 0, 0};

  {
    const u16* x = br1 ? xB : xA;
    const u16* W = br1 ? wB : wA;
    const int tt = w & 3, hp = w >> 2;
    const uint32_t xo0 = (uint32_t)((ti1 * 128 + 16 * w + (lane >> 3)) * DM + (((lane & 7) ^ ((lane >> 4) & 7)) * 8)) * 2u;
    const uint32_t xo1 = (uint32_t)((ti1 * 128 + 16 * w + 8 + (lane >> 3)) * DM + (((lane & 7) ^ ((4 + (lane >> 4)) & 7)) * 8)) * 2u;
    const uint32_t wo0 = (uint32_t)((8 * w + (lane >> 3)) * 1536 + g1 * 64 + (((lane & 7) ^ (((lane >> 4) & 1) << 2)) * 8)) * 2u;
    const int s0 = ((idx & 7) + 2 * (idx >> 3)) & 7;
    auto stage_load = [&](int su, int buf, int part) {
      const int s = (su + s0) & 7;
      const uint32_t b = sm0 + buf * STAGE_BYTES;
      if (part == 0) {
        glds16s(x + s * 64, xo0, b + w * 2048);
        glds16s(x + s * 64, xo1, b + w * 2048 + 1024);
      } else if (part == 1) {
        glds16s(W + (size_t)s * 64 * 1536, wo0, b + 16384 + w * 1024);
        glds16s(W + (size_t)s * 64 * 1536 + 512, wo0, b + 16384 + 8192 + w * 1024);
      } else {
        glds16s(W + (size_t)s * 64 * 1536 + 1024, wo0, b + 16384 + 16384 + w * 1024);
      }
    };
    asm volatile("" ::"s"(out), "s"(ws), "s"(woA), "s"(woB));
    for (int p = 0; p < 3; ++p) stage_load(0, 0, p);
    for (int p = 0; p < 3; ++p) stage_load(1, 1, p);
    if (w == 1) glds16s(wphi + (size_t)(head & ~1) * 256, (uint32_t)lane * 16u, sm0 + L_WPHI2);
    if (br1 == 0 && w >= 6) glds16s(wphi + (size_t)(4 * g1) * 256 + (w - 6) * 512, (uint32_t)lane * 16u, sm0 + L_WPHI + (w - 6) * 1024);
    {
      const int cc = (ti1 >> 1) + (lane >> 2);
      const int j = lane - 32, tj = 2 * chunk + ((j >> 3) & 1);
      const int c3 = ((j & 7) * 4 + (tj & 3)) * 8 + (j >> 4) * 4 + (tj >> 2);
      if (lane >= 32) { pact = true; pslot = c3 * 64 + 32 + head; }
      else if (br1) { pact = lane < 8 && cc <= 7; pslot = (cc * 32 + 4 * g1 + (lane & 3)) * 64 + (ti1 - 2 * cc + 2); }
      else { pact = true; pslot = ((lane >> 2) * 32 + 4 * g1 + (lane & 3)) * 64 + 16 + ti1; }
      if (!pact) pslot = cslot;
    }
    cwant = __hip_atomic_load((const gu64*)(ws + WS_E) + cslot, RLX_AGENT);
    pval = __hip_atomic_load((const gu64*)(ws + WS_E) + pslot, RLX_AGENT);

    const int xrow = 32 * tt + (lane & 31);
    const int xa0 = xrow * 128 + ((h ^ ((xrow >> 1) & 7)) * 16);
    const int q4 = (lane & 15) >> 2, p4 = lane & 3;
    const int wa0 = 16384 + (8 * h + q4) * 128 + (((4 * hp + 2 * ((lane >> 4) & 1) + (p4 >> 1)) ^ (((q4 >> 1) & 1) << 2)) * 16) + 8 * (p4 & 1);

    f32x16 aq = zero16(), ak = zero16(), av = zero16();
    bf16x8 xf[2], fq[2], fk[2], fv[2];
    auto frags = [&](const char* b, int ks, int p) {
      xf[p] = lds_read16(b + (xa0 ^ (32 * ks)));
      const char* wb = b + wa0 + ks * 2048;
      fq[p] = tr_frag(wb, wb + 512);
      fk[p] = tr_frag(wb + 8192, wb + 8192 + 512);
      fv[p] = tr_frag(wb + 16384, wb + 16384 + 512);
    };
    auto mma = [&](int p) {
      aq = MFMA32(fq[p], xf[p], aq);
      ak = MFMA32(fk[p], xf[p], ak);
      av = MFMA32(xf[p], fv[p], av);
    };
#pragma unroll
    for (int s = 0; s < 8; ++s) {
      if (s == 0) asm volatile("s_waitcnt vmcnt(7)" ::: "memory");
      else if (s < 7) asm volatile("s_waitcnt vmcnt(5)" ::: "memory");
      else asm volatile("s_waitcnt vmcnt(0)" ::: "memory");
      __builtin_amdgcn_s_barrier();
      asm volatile("" ::: "memory");
      if (s == 0) STAMP(1);
      const char* b = smem + (s % 3) * STAGE_BYTES;
      frags(b, 0, 0);
      if (s + 2 < 8) stage_load(s + 2, (s + 2) % 3, 0);
      __builtin_amdgcn_sched_barrier(0);
      if (s > 0) mma(1);
      __builtin_amdgcn_sched_barrier(0);
      frags(b, 1, 1);
      if (s + 2 < 8) stage_load(s + 2, (s + 2) % 3, 1);
      __builtin_amdgcn_sched_barrier(0);
      mma(0);
      __builtin_amdgcn_sched_barrier(0);
      frags(b, 2, 0);
      if (s + 2 < 8) stage_load(s + 2, (s + 2) % 3, 2);
      __builtin_amdgcn_sched_barrier(0);
      mma(1);
      __builtin_amdgcn_sched_barrier(0);
      frags(b, 3, 1);
      __builtin_amdgcn_sched_barrier(0);
      mma(0);
      __builtin_amdgcn_sched_barrier(0);
      asm volatile("s_waitcnt lgkmcnt(0)" ::: "memory");
    }
    mma(1);
    STAMP(2);
    const int tile32 = ti1 * 4 + tt, head0 = 4 * g1 + 2 * hp, blk = (lane >> 4) & 1;
    if (br1) {
      const uint32_t qo = (head0 * NT + tile32) * 1024 + lane * 16;
      st_sc1_frag(wsr, WS_QB + qo, pack8s<0>(aq, 0.25f * L2E));
      st_sc1_frag(wsr, WS_QB + qo + NT * 1024, pack8s<8>(aq, 0.25f * L2E));
      st_sc1_frag(wsr, WS_KB + qo, pack8<0>(ak));
      st_sc1_frag(wsr, WS_KB + qo + NT * 1024, pack8<8>(ak));
      const uint32_t vo = WS_VB + ((head0 + ((lane >> 4) & 1)) * NT + tile32) * 1024 + h * 256 + (lane & 15) * 16;
      st_sc1_frag(wsr, vo, pack8<0>(av));
      st_sc1_frag(wsr, vo + 512, pack8<8>(av));
    } else {
      const uint32_t qo = WS_QA + (head0 * NT + tile32) * 1024 + lane * 16;
      st_sc1_frag(wsr, qo, pack8<0>(aq));
      st_sc1_frag(wsr, qo + NT * 1024, pack8<8>(aq));
      s16x8 i0, i1;
#pragma unroll
      for (int j = 0; j < 8; ++j) {
        const int f = 8 * (j >> 2) + 4 * h + (j & 3);
        i0[j] = ((lane & 31) == f) ? (short)0x3F80 : (short)0;
        i1[j] = ((lane & 31) == 16 + f) ? (short)0x3F80 : (short)0;
      }
      const bf16x8 id0 = __builtin_bit_cast(bf16x8, i0), id1 = __builtin_bit_cast(bf16x8, i1);
      const u32x4 ones = {0x3F803F80u, 0x3F803F80u, 0x3F803F80u, 0x3F803F80u};
      const u32x4 vf0 = __builtin_bit_cast(u32x4, pack8<0>(av)), vf1 = __builtin_bit_cast(u32x4, pack8<8>(av));
      f32x16 pk0 = MFMA32(wphi_frag_lds(smem + L_WPHI + (2 * hp) * 512, lane), pack8<0>(ak), zero16());
      f32x16 pk1 = MFMA32(wphi_frag_lds(smem + L_WPHI + (2 * hp + 1) * 512, lane), pack8<8>(ak), zero16());
      STAMP(13);
      bf16x8 fp[2], fn[2];
      dual_softmax(pk0, fp[0], fn[0]);
      dual_softmax(pk1, fp[1], fn[1]);
      STAMP(14);
#pragma unroll
      for (int p = 0; p < 2; ++p) {
        f32x16 T = MFMA32(fp[p], id0, zero16());
        T = MFMA32(fn[p], id1, T);
        const bool mine = blk == p;
        f32x16 kvp = MFMA32(pack8<0>(T), __builtin_bit_cast(bf16x8, mine ? vf0 : ones), zero16());
        kvp = MFMA32(pack8<8>(T), __builtin_bit_cast(bf16x8, mine ? vf1 : ones), kvp);
        if (mine || (lane & 15) == 0) {
          char* dst = smem + ((2 * hp + p) * 4 + tt) * 2176 + (mine ? (h * 16 + (lane & 15)) * 16 : 2048 + h * 16);
          const int st = mine ? 512 : 32;
#pragma unroll
          for (int r4 = 0; r4 < 4; ++r4) {
            f32x4 v = {kvp[4 * r4], kvp[4 * r4 + 1], kvp[4 * r4 + 2], kvp[4 * r4 + 3]};
            *(f32x4*)(dst + r4 * st) = v;
          }
        }
      }
      wg_barrier();
      STAMP(15);
      if (w < 4 || (w == 4 && lane < 16)) {
        const int hh = w < 4 ? w : (lane >> 2);
        const char* src = smem + hh * 4 * 2176 + (w < 4 ? (lane >> 5) * 1024 + (lane & 31) * 16 : 2048 + (lane & 3) * 32);
        const int d2 = w < 4 ? 512 : 16;
        f32x4 s0v = *(const f32x4*)src, s1v = *(const f32x4*)(src + d2);
#pragma unroll
        for (int t = 1; t < 4; ++t) {
          s0v += *(const f32x4*)(src + t * 2176);
          s1v += *(const f32x4*)(src + t * 2176 + d2);
        }
        bf16x8 o;
#pragma unroll
        for (int c = 0; c < 4; ++c) { o[c] = (__bf16)s0v[c]; o[4 + c] = (__bf16)s1v[c]; }
        st_sc1_frag(wsr, WS_KVP + ((4 * g1 + hh) * 16 + ti1) * KVP_BYTES + (w < 4 ? lane * 16 : 1024 + (lane & 3) * 16), o);
      }
    }
    u64 first = 0;
    if (w == 0) first = poll_slot(ws, bid * 64 + (lane & 3));
    drain_barrier();
    STAMP(3);
    cwant = mix(cwant);
    if (w == 0) {
      pval = mix(pval);
      signal_slots(ws, pslot, pval, pact && lane < 32);
      wait_slots(ws, bid * 64 + (lane & 3), cwant, lane < 4 && 2 * chunk - 2 + lane >= 0, first, polls);
    }
    if (tid >= 448) {
      const u32x4 ones = {0x3F803F80u, 0x3F803F80u, 0x3F803F80u, 0x3F803F80u};
      *(u32x4*)(smem + L_ONES + (tid - 448) * 16) = ones;
    }
    wg_barrier();
    STAMP(4);
  }

  const int qt = chunk * 8 + w;
  const bool lower = (lane & 16) == 0;
  bf16x8 qf;
  {
    const int t0 = chunk * 8 - 8;
    u32x4 kv[4];
    if (t0 + 2 * w >= 0) {
      const uint32_t o = (head * NT + t0 + 2 * w) * 1024 + lane * 16;
      kv[0] = ld_sc1(wsr, WS_KB + o);
      kv[1] = ld_sc1(wsr, WS_KB + o + 1024);
      kv[2] = ld_sc1(wsr, WS_VB + o);
      kv[3] = ld_sc1(wsr, WS_VB + o + 1024);
    }
    qf = ld_sc1_frag(wsr, WS_QB + (head * NT + qt) * 1024 + lane * 16);
    if (t0 + 2 * w >= 0) {
      *(u32x4*)(smem + L_K + 2 * w * 1024 + lane * 16) = kv[0];
      *(u32x4*)(smem + L_K + 2 * w * 1024 + 1024 + lane * 16) = kv[1];
      *(u32x4*)(smem + L_V + 2 * w * 1024 + lane * 16) = kv[2];
      *(u32x4*)(smem + L_V + 2 * w * 1024 + 1024 + lane * 16) = kv[3];
    }
    poll_retire(polls);
    wg_barrier();
    STAMP(5);
  }
  const bool aslot = lane >= 16 && lane < 32;
  const u64 early = __hip_atomic_load((const gu64*)(ws + WS_F) + cslot, RLX_AGENT);
  u32x4 pp[2], pz;
  bf16x8 qa;
  const u16* wo = br3 ? woB : woA;
  auto wo_load = [&]() {
    const uint32_t off = (uint32_t)((64 * w + (lane >> 3)) * DM + nj * 64 + (((lane & 7) ^ (((lane >> 4) & 1) << 2)) * 8)) * 2u;
#pragma unroll
    for (int i = 0; i < 8; ++i) glds16s(wo + i * 8 * DM, off, sm0 + L_WO + w * 8192 + i * 1024);
  };

  {
    const char* kbase = smem + L_K + w * 1024 + lane * 16;
    const int vinc = lower ? 1024 : 0;
    const char* vbase = lower ? (smem + L_V + w * 1024 + h * 256 + (lane & 15) * 16) : (smem + L_ONES + (lane & 15) * 16);
    const int ql = lane & 31;
    const int first = chunk > 0 ? 0 : 8 - w;
    auto a_fetch = [&]() {
      if (__builtin_amdgcn_ballot_w64(aslot && early != cwant) != 0) spin_slots(ws, cslot, cwant, aslot);
      asm volatile("" ::: "memory");
      if (w == 0 && aslot) __hip_atomic_store((gu64*)(ws + WS_E) + cslot, cwant, RLX_AGENT);
      const uint32_t kb = WS_KVP + (head * 16 + 2 * w) * KVP_BYTES;
      pp[0] = ld_sc1(wsr, kb + lane * 16);
      pp[1] = ld_sc1(wsr, kb + KVP_BYTES + lane * 16);
      pz = ld_sc1(wsr, kb + ((lane >> 2) & 1) * KVP_BYTES + 1024 + (lane & 3) * 16);
      qa = ld_sc1_frag(wsr, WS_QA + (head * NT + qt) * 1024 + lane * 16);
    };
    auto a_readout = [&]() {
      f32x4 s0v, s1v, z0, z1;
      s0v[0] = __uint_as_float(pp[0][0] << 16) + __uint_as_float(pp[1][0] << 16);
      s0v[1] = __uint_as_float(pp[0][0] & 0xFFFF0000u) + __uint_as_float(pp[1][0] & 0xFFFF0000u);
      s0v[2] = __uint_as_float(pp[0][1] << 16) + __uint_as_float(pp[1][1] << 16);
      s0v[3] = __uint_as_float(pp[0][1] & 0xFFFF0000u) + __uint_as_float(pp[1][1] & 0xFFFF0000u);
      s1v[0] = __uint_as_float(pp[0][2] << 16) + __uint_as_float(pp[1][2] << 16);
      s1v[1] = __uint_as_float(pp[0][2] & 0xFFFF0000u) + __uint_as_float(pp[1][2] & 0xFFFF0000u);
      s1v[2] = __uint_as_float(pp[0][3] << 16) + __uint_as_float(pp[1][3] << 16);
      s1v[3] = __uint_as_float(pp[0][3] & 0xFFFF0000u) + __uint_as_float(pp[1][3] & 0xFFFF0000u);
      z0[0] = __uint_as_float(pz[0] << 16); z0[1] = __uint_as_float(pz[0] & 0xFFFF0000u);
      z0[2] = __uint_as_float(pz[1] << 16); z0[3] = __uint_as_float(pz[1] & 0xFFFF0000u);
      z1[0] = __uint_as_float(pz[2] << 16); z1[1] = __uint_as_float(pz[2] & 0xFFFF0000u);
      z1[2] = __uint_as_float(pz[3] << 16); z1[3] = __uint_as_float(pz[3] & 0xFFFF0000u);
#pragma unroll
      for (int c = 0; c < 4; ++c) { z0[c] += __shfl_xor(z0[c], 4); z1[c] += __shfl_xor(z1[c], 4); }
      char* red = smem + L_RED;
      *(f32x4*)(red + w * 2048 + lane * 16) = s0v;
      *(f32x4*)(red + w * 2048 + 1024 + lane * 16) = s1v;
      if (lane < 4) {
        *(f32x4*)(red + 16384 + w * 128 + lane * 32) = z0;
        *(f32x4*)(red + 16384 + w * 128 + lane * 32 + 16) = z1;
      }
      wo_load();
      const bf16x8 wf = wphi_frag_lds(smem + L_WPHI2 + (head & 1) * 512, lane);
      const f32x16 pq = MFMA32(wf, qa, zero16());
      wg_barrier();
      STAMP(7);
      if (w == 0) {
        const char* src = red + lane * 16;
        f32x4 t0 = *(const f32x4*)src, t1 = *(const f32x4*)(src + 1024);
#pragma unroll
        for (int t = 1; t < 8; ++t) { t0 += *(const f32x4*)(src + t * 2048); t1 += *(const f32x4*)(src + t * 2048 + 1024); }
        bf16x8 o;
#pragma unroll
        for (int c = 0; c < 4; ++c) { o[c] = (__bf16)t0[c]; o[4 + c] = (__bf16)t1[c]; }
        *(bf16x8*)(smem + L_KVF + (lane >> 5) * 1024 + (((lane >> 4) & 1) * 32 + (lane & 15)) * 16) = o;
      }
      if (w == 1) {
        const int f = lane & 31;
        const float* zr = (const float*)(red + 16384);
        const __bf16 z = (__bf16)(((zr[f] + zr[32 + f]) + (zr[64 + f] + zr[96 + f])) + ((zr[128 + f] + zr[160 + f]) + (zr[192 + f] + zr[224 + f])));
#pragma unroll
        for (int i = 0; i < 8; ++i)
          *(__bf16*)(smem + L_KVF + (f >> 4) * 1024 + (((f >> 2) & 1) * 32 + 16 + 8 * h + i) * 16 + (((f >> 3) & 1) * 4 + (f & 3)) * 2) = z;
      }
      bf16x8 fp, fn;
      dual_softmax(pq, fp, fn);
      wg_barrier();
      f32x16 oa = MFMA32(lds_read16(smem + L_KVF + lane * 16), fp, zero16());
      oa = MFMA32(lds_read16(smem + L_KVF + 1024 + lane * 16), fn, oa);
      const float inv = __builtin_amdgcn_rcpf(oa[8] + 1e-6f);
      st_sc1_frag(wsr, WS_O + (head * NT + qt) * 1024 + lane * 16, pack8s<0>(oa, inv));
      STAMP(8);
    };
    auto boundary_p = [&](const f32x16& sm, float m, u32x4& da, u32x4& db, u32x4& ua, u32x4& ub) {
      f32x16 e;
#pragma unroll
      for (int r = 0; r < 16; ++r) e[r] = __builtin_amdgcn_exp2f(sm[r] - m);
      const u32x4 pa = __builtin_bit_cast(u32x4, pack8<0>(e)), pb = __builtin_bit_cast(u32x4, pack8<8>(e));
#pragma unroll
      for (int i = 0; i < 4; ++i) {
        const int ra = 2 * i, rb = 8 + 2 * i;
        const int ka = (ra & 3) + 8 * (ra >> 2) + 4 * h, kb = (rb & 3) + 8 * (rb >> 2) + 4 * h;
        const unsigned ma = (ka <= ql ? 0xFFFFu : 0u) | (ka + 1 <= ql ? 0xFFFF0000u : 0u);
        const unsigned mb = (kb <= ql ? 0xFFFFu : 0u) | (kb + 1 <= ql ? 0xFFFF0000u : 0u);
        da[i] = pa[i] & ma; ua[i] = pa[i] & ~ma;
        db[i] = pb[i] & mb; ub[i] = pb[i] & ~mb;
      }
    };
    f32x16 o = zero16();
    if (chunk > 0) {
      bf16x8 kf[9];
      kf[8] = lds_read16(kbase + 8 * 1024);
      kf[0] = lds_read16(kbase);
      kf[1] = lds_read16(kbase + 1024);
      kf[2] = lds_read16(kbase + 2048);
      f32x16 sm = MFMA32(kf[8], qf, zero16());
      f32x16 s0 = MFMA32(kf[0], qf, zero16());
      f32x16 sc = MFMA32(kf[1], qf, zero16());
#pragma unroll
      for (int r = 0; r < 16; ++r) {
        const int kl = (r & 3) + 8 * (r >> 2) + 4 * h;
        sm[r] = (kl <= ql) ? sm[r] : s0[r];
      }
      float m = sm[0];
#pragma unroll
      for (int r = 1; r < 16; ++r) m = fmaxf(m, sm[r]);
#pragma unroll
      for (int i = 1; i < 8; ++i) {
        f32x16 sn;
        if (i + 2 < 8) kf[i + 2] = lds_read16(kbase + (i + 2) * 1024);
        if (i < 7) sn = MFMA32(kf[i + 1], qf, zero16());
        __builtin_amdgcn_sched_barrier(0);
#pragma unroll
        for (int r = 0; r < 16; r += 2) m = fmaxf(fmaxf(m, sc[r]), sc[r + 1]);
        __builtin_amdgcn_sched_barrier(0);
        if (i < 7) sc = sn;
      }
      m = swap_max(m);
      a_fetch();
      f32x16 negm;
#pragma unroll
      for (int r = 0; r < 16; ++r) negm[r] = -m;
      sc = MFMA32(kf[1], qf, negm);
      {
        const bf16x8 vd0 = lds_read16(vbase + 8 * vinc), vd1 = lds_read16(vbase + 8 * vinc + 512);
        const bf16x8 vu0 = lds_read16(vbase), vu1 = lds_read16(vbase + 512);
        u32x4 da, db, ua, ub;
        boundary_p(sm, m, da, db, ua, ub);
        o = MFMA32(vd0, __builtin_bit_cast(bf16x8, da), o);
        o = MFMA32(vd1, __builtin_bit_cast(bf16x8, db), o);
        o = MFMA32(vu0, __builtin_bit_cast(bf16x8, ua), o);
        o = MFMA32(vu1, __builtin_bit_cast(bf16x8, ub), o);
      }
#pragma unroll
      for (int i = 1; i < 8; ++i) {
        const bf16x8 v0 = lds_read16(vbase + i * vinc), v1 = lds_read16(vbase + i * vinc + 512);
        f32x16 sn, e;
        __builtin_amdgcn_sched_barrier(0);
#pragma unroll
        for (int r = 0; r < 4; ++r) e[r] = __builtin_amdgcn_exp2f(sc[r]);
        __builtin_amdgcn_sched_barrier(0);
        if (i < 7) sn = MFMA32(kf[i + 1], qf, negm);
        __builtin_amdgcn_sched_barrier(0);
#pragma unroll
        for (int r = 4; r < 8; ++r) e[r] = __builtin_amdgcn_exp2f(sc[r]);
        const bf16x8 p0 = pack8<0>(e);
#pragma unroll
        for (int r = 8; r < 10; ++r) e[r] = __builtin_amdgcn_exp2f(sc[r]);
        __builtin_amdgcn_sched_barrier(0);
        o = MFMA32(v0, p0, o);
        __builtin_amdgcn_sched_barrier(0);
#pragma unroll
        for (int r = 10; r < 16; ++r) e[r] = __builtin_amdgcn_exp2f(sc[r]);
        const bf16x8 p1 = pack8<8>(e);
        __builtin_amdgcn_sched_barrier(0);
        o = MFMA32(v1, p1, o);
        __builtin_amdgcn_sched_barrier(0);
        if (i < 7) sc = sn;
        if (i == A_AT) { a_readout(); __builtin_amdgcn_sched_barrier(0); }
      }
    } else {
      f32x16 sm = MFMA32(lds_read16(kbase + 8 * 1024), qf, zero16());
#pragma unroll
      for (int r = 0; r < 16; ++r) {
        const int kl = (r & 3) + 8 * (r >> 2) + 4 * h;
        sm[r] = (kl <= ql) ? sm[r] : -INFINITY;
      }
      float m = sm[0];
#pragma unroll
      for (int r = 1; r < 16; ++r) m = fmaxf(m, sm[r]);
#pragma unroll 1
      for (int i = first; i < 8; ++i) {
        const f32x16 s = MFMA32(lds_read16(kbase + i * 1024), qf, zero16());
#pragma unroll
        for (int r = 0; r < 16; r += 2) m = fmaxf(fmaxf(m, s[r]), s[r + 1]);
      }
      m = swap_max(m);
      a_fetch();
      u32x4 da, db, ua, ub;
      boundary_p(sm, m, da, db, ua, ub);
      o = MFMA32(lds_read16(vbase + 8 * vinc), __builtin_bit_cast(bf16x8, da), o);
      o = MFMA32(lds_read16(vbase + 8 * vinc + 512), __builtin_bit_cast(bf16x8, db), o);
#pragma unroll 1
      for (int i = first; i < 8; ++i) {
        const f32x16 s = MFMA32(lds_read16(kbase + i * 1024), qf, zero16());
        f32x16 e;
#pragma unroll
        for (int r = 0; r < 16; ++r) e[r] = __builtin_amdgcn_exp2f(s[r] - m);
        o = MFMA32(lds_read16(vbase + i * vinc), pack8<0>(e), o);
        o = MFMA32(lds_read16(vbase + i * vinc + 512), pack8<8>(e), o);
      }
      a_readout();
    }
    const float inv = __builtin_amdgcn_rcpf(o[8]);
    st_sc1_frag(wsr, WS_O + FRAG_BYTES + (head * NT + qt) * 1024 + lane * 16, pack8s<0>(o, inv));
    STAMP(6);
    STAMPW(16);
    drain_barrier();
    STAMP(9);
    if (w == 0) {
      signal_slots(ws, pslot, pval, lane >= 32);
      wait_slots(ws, cslot | 32, cwant, lane >= 32, ~cwant, polls);
    }
    wg_barrier();
    STAMP(10);
  }

  {
    const int tt = w & 3, kh = w >> 2;
    const int tile32 = ti3 * 4 + tt;
    const uint32_t og = WS_O + br3 * FRAG_BYTES + ((16 * kh) * NT + tile32) * 1024 + lane * 16;
    bf16x8 of[16];
#pragma unroll
    for (int i = 0; i < 16; ++i) of[i] = ld_sc1_frag(wsr, og + i * NT * 1024);
    __builtin_amdgcn_sched_barrier(0);
    const int blk = (lane >> 4) & 1, q4 = (lane & 15) >> 2, p4 = lane & 3;
    f32x16 acc0 = zero16(), acc1 = zero16();
    const char* wl = smem + L_WO;
    const int ra0 = (256 * kh + 4 * h + q4) * 128 + (((2 * blk + (p4 >> 1)) ^ (((q4 >> 1) & 1) << 2)) * 16) + 8 * (p4 & 1);
    const int ra1 = (256 * kh + 4 * h + q4) * 128 + (((4 + 2 * blk + (p4 >> 1)) ^ (((q4 >> 1) & 1) << 2)) * 16) + 8 * (p4 & 1);
#pragma unroll
    for (int i = 0; i < 16; ++i) {
      const int kb = 16 * i * 128;
      const bf16x8 a0 = tr_frag(wl + ra0 + kb, wl + ra0 + kb + 1024);
      const bf16x8 a1 = tr_frag(wl + ra1 + kb, wl + ra1 + kb + 1024);
      acc0 = MFMA32(a0, of[i], acc0);
      acc1 = MFMA32(a1, of[i], acc1);
    }
    poll_retire(polls);
    STAMP(11);
    float* ex = (float*)smem + w * 1024 + lane * 4;
    const f32x16 give = kh ? acc0 : acc1;
    f32x16 keep = kh ? acc1 : acc0;
#pragma unroll
    for (int r4 = 0; r4 < 4; ++r4) {
      f32x4 v = {give[4 * r4], give[4 * r4 + 1], give[4 * r4 + 2], give[4 * r4 + 3]};
      *(f32x4*)(ex + r4 * 256) = v;
    }
    wg_barrier();
    const float* px = (const float*)smem + (w ^ 4) * 1024 + lane * 4;
#pragma unroll
    for (int r4 = 0; r4 < 4; ++r4) {
      const f32x4 v = *(const f32x4*)(px + r4 * 256);
#pragma unroll
      for (int c = 0; c < 4; ++c) keep[4 * r4 + c] += v[c];
    }
    const u32x4 lo = __builtin_bit_cast(u32x4, pack8<0>(keep)), hi = __builtin_bit_cast(u32x4, pack8<8>(keep));
    u16* op = out + (size_t)(tile32 * 32 + (lane & 31)) * 1024 + br3 * 512 + nj * 64 + kh * 32 + h * 8;
    auto t0 = __builtin_amdgcn_permlane32_swap(lo[0], lo[2], false, false);
    auto t1 = __builtin_amdgcn_permlane32_swap(lo[1], lo[3], false, false);
    u32x4 v = {t0[0], t1[0], t0[1], t1[1]};
    OUT_ST(op, v);
    auto t2 = __builtin_amdgcn_permlane32_swap(hi[0], hi[2], false, false);
    auto t3 = __builtin_amdgcn_permlane32_swap(hi[1], hi[3], false, false);
    u32x4 u = {t2[0], t3[0], t2[1], t3[1]};
    OUT_ST(op + 16, u);
  }
  STAMP(12);
}

extern "C" void kernel_launch(void* const* d_in, const int* in_sizes, int n_in, void* d_out, int out_size,
                              void* d_ws, size_t ws_size, hipStream_t stream) {
  fused<<<256, 512, 0, stream>>>((const u16*)d_in[0], (const u16*)d_in[1], (const u16*)d_in[2], (const u16*)d_in[3],
                                 (const u16*)d_in[4], (const u16*)d_in[5], (const u16*)d_in[6], (char*)d_ws, (u16*)d_out);
}
```

```cpp
#include <hip/hip_runtime.h>
#include <hip/hip_bf16.h>
#include <stdint.h>

typedef __bf16 bf16x8 __attribute__((ext_vector_type(8)));
typedef __bf16 bf16x4 __attribute__((ext_vector_type(4)));
typedef short s16x4 __attribute__((ext_vector_type(4)));
typedef short s16x8 __attribute__((ext_vector_type(8)));
typedef float f32x16 __attribute__((ext_vector_type(16)));
typedef float f32x4 __attribute__((ext_vector_type(4)));
typedef unsigned u32x4 __attribute__((ext_vector_type(4)));
typedef unsigned u32x2 __attribute__((ext_vector_type(2)));
typedef unsigned short u16;

#define LDS_AS __attribute__((address_space(3)))
#define GLB_AS __attribute__((address_space(1)))
#define DEVI __device__ __forceinline__
#define MFMA32(a, b, c) __builtin_amdgcn_mfma_f32_32x32x16_bf16((a), (b), (c), 0, 0, 0)

constexpr int SEQ = 2048, DM = 512, NH = 32, NT = 64;
constexpr float L2E = 1.4426950408889634f;

constexpr uint32_t FRAG_BYTES = NH * NT * 1024;
constexpr uint32_t WS_QA = 0, WS_QB = FRAG_BYTES, WS_KB = 2 * FRAG_BYTES, WS_VB = 3 * FRAG_BYTES;
constexpr uint32_t WS_O = 4 * FRAG_BYTES;
constexpr uint32_t WS_KVP = 6 * FRAG_BYTES;
constexpr uint32_t KVP_BYTES = 1088;
constexpr uint32_t WS_E = WS_KVP + NH * 16 * KVP_BYTES;
constexpr uint32_t WS_F = WS_E + 256 * 64 * 8;
constexpr uint32_t WS_END = WS_F + 256 * 64 * 8;

DEVI void glds16s(const void* sbase, uint32_t voff, uint32_t lds_addr) {
  asm volatile("s_mov_b32 m0, %2\n\ts_nop 0\n\tglobal_load_lds_dwordx4 %0, %1" ::"v"(voff), "s"(sbase), "s"(lds_addr) : "memory");
}
DEVI void glds4s(const void* sbase, uint32_t voff, uint32_t lds_addr) {
  asm volatile("s_mov_b32 m0, %2\n\ts_nop 0\n\tglobal_load_lds_dword %0, %1" ::"v"(voff), "s"(sbase), "s"(lds_addr) : "memory");
}
DEVI uint32_t lds_addr_of(const char* p) { return (uint32_t)(uintptr_t)(const LDS_AS char*)p; }
DEVI bf16x8 lds_read16(const char* p) { return *(const bf16x8*)p; }
DEVI bf16x8 tr_frag(const char* p0, const char* p1) {
  s16x4 a = __builtin_amdgcn_ds_read_tr16_b64_v4i16((LDS_AS s16x4*)p0);
  s16x4 b = __builtin_amdgcn_ds_read_tr16_b64_v4i16((LDS_AS s16x4*)p1);
  s16x8 r = __builtin_shufflevector(a, b, 0, 1, 2, 3, 4, 5, 6, 7);
  return __builtin_bit_cast(bf16x8, r);
}
DEVI float max2(float a, float b) { return __builtin_elementwise_maximum(a, b); }
DEVI float max3(float a, float b, float c) { return max2(max2(a, b), c); }
DEVI float swap_max(float x) {
  auto t = __builtin_amdgcn_permlane32_swap(__float_as_uint(x), __float_as_uint(x), false, false);
  return max2(__uint_as_float(t[0]), __uint_as_float(t[1]));
}
DEVI float swap_add(float x) {
  auto t = __builtin_amdgcn_permlane32_swap(__float_as_uint(x), __float_as_uint(x), false, false);
  return __uint_as_float(t[0]) + __uint_as_float(t[1]);
}
typedef float f32x2 __attribute__((ext_vector_type(2)));
typedef __bf16 bf16x2 __attribute__((ext_vector_type(2)));
DEVI unsigned cvt2(float a, float b) {
  const f32x2 v = {a, b};
  return __builtin_bit_cast(unsigned, __builtin_convertvector(v, bf16x2));
}
template <int O>
DEVI bf16x8 pack8(const f32x16& a) {
  const u32x4 r = {cvt2(a[O], a[O + 1]), cvt2(a[O + 2], a[O + 3]), cvt2(a[O + 4], a[O + 5]), cvt2(a[O + 6], a[O + 7])};
  return __builtin_bit_cast(bf16x8, r);
}
template <int O>
DEVI bf16x8 pack8s(const f32x16& a, float s) {
  const u32x4 r = {cvt2(a[O] * s, a[O + 1] * s), cvt2(a[O + 2] * s, a[O + 3] * s), cvt2(a[O + 4] * s, a[O + 5] * s),
                   cvt2(a[O + 6] * s, a[O + 7] * s)};
  return __builtin_bit_cast(bf16x8, r);
}
DEVI f32x16 zero16() {
  f32x16 z;
#pragma unroll
  for (int i = 0; i < 16; ++i) z[i] = 0.f;
  return z;
}
DEVI bf16x8 wphi_frag_lds(const char* wl, int lane) {
  const char* p = wl + (4 * (lane >> 5) + ((lane & 15) >> 2)) * 32 + (lane & 3) * 8;
  const u32x4 r = __builtin_bit_cast(u32x4, tr_frag(p, p + 256));
  const unsigned sgn = (lane & 16) ? 0x80008000u : 0u;
  const u32x4 o = {r[0] ^ sgn, r[1] ^ sgn, r[2] ^ sgn, r[3] ^ sgn};
  return __builtin_bit_cast(bf16x8, o);
}
DEVI void dual_softmax(const f32x16& p, bf16x8& fp, bf16x8& fn) {
  float mp = max3(max3(p[0], p[1], p[2]), max3(p[3], p[4], p[5]), max2(p[6], p[7]));
  float mn = max3(max3(p[8], p[9], p[10]), max3(p[11], p[12], p[13]), max2(p[14], p[15]));
  mp = swap_max(mp) * L2E;
  mn = swap_max(mn) * L2E;
  f32x16 e;
  float sp = 0.f, sn = 0.f;
#pragma unroll
  for (int j = 0; j < 8; ++j) {
    e[j] = __builtin_amdgcn_exp2f(__builtin_fmaf(p[j], L2E, -mp));
    e[8 + j] = __builtin_amdgcn_exp2f(__builtin_fmaf(p[8 + j], L2E, -mn));
    sp += e[j];
    sn += e[8 + j];
  }
  sp = __builtin_amdgcn_rcpf(swap_add(sp));
  sn = __builtin_amdgcn_rcpf(swap_add(sn));
  fp = pack8s<0>(e, sp);
  fn = pack8s<8>(e, sn);
}

typedef unsigned long long u64;
typedef GLB_AS u64 gu64;
#define RLX_AGENT __ATOMIC_RELAXED, __HIP_MEMORY_SCOPE_AGENT
constexpr int AUX_SC1 = 16;
DEVI u32x4 ld_sc1(__amdgpu_buffer_rsrc_t r, uint32_t off) { return __builtin_amdgcn_raw_buffer_load_b128(r, off, 0, AUX_SC1); }
DEVI bf16x8 ld_sc1_frag(__amdgpu_buffer_rsrc_t r, uint32_t off) { return __builtin_bit_cast(bf16x8, ld_sc1(r, off)); }
DEVI void st_sc1(__amdgpu_buffer_rsrc_t r, uint32_t off, u32x4 v) { __builtin_amdgcn_raw_buffer_store_b128(v, r, off, 0, AUX_SC1); }
DEVI void st_sc1_frag(__amdgpu_buffer_rsrc_t r, uint32_t off, bf16x8 v) { st_sc1(r, off, __builtin_bit_cast(u32x4, v)); }
DEVI void wg_barrier() {
  asm volatile("s_waitcnt lgkmcnt(0)" ::: "memory");
  __builtin_amdgcn_s_barrier();
  asm volatile("" ::: "memory");
}
DEVI void drain_barrier() {
  asm volatile("s_waitcnt vmcnt(0) lgkmcnt(0)" ::: "memory");
  __builtin_amdgcn_s_barrier();
  asm volatile("" ::: "memory");
}
constexpr int POLL_GAP = 8;
DEVI u64 mix(u64 e) { return e * 0x9E3779B97F4A7C15ull + 0x632BE59BD9B4E019ull; }
struct Polls { u64 a, b, c; };
DEVI u64 poll_slot(char* ws, uint32_t poll) { return __hip_atomic_load((const gu64*)(ws + WS_F) + poll, RLX_AGENT); }
#define POLL_ISSUE(r) asm volatile("global_load_dwordx2 %0, %1, %2 sc1" : "=v"(r) : "v"(off), "s"(base) : "memory")
#define POLL_WAIT(r) asm volatile("s_waitcnt vmcnt(2)" : "+v"(r)::"memory")
DEVI void wait_slots(char* ws, uint32_t poll, u64 want, bool active, u64 first, Polls& p) {
  if (__builtin_amdgcn_ballot_w64(active && first != want) != 0) {
    const char* base = ws + WS_F;
    const uint32_t off = poll * 8u;
    POLL_ISSUE(p.a);
    __builtin_amdgcn_s_sleep(POLL_GAP);
    POLL_ISSUE(p.b);
    __builtin_amdgcn_s_sleep(POLL_GAP);
    POLL_ISSUE(p.c);
#pragma unroll 1
    for (int it = 0; it < 100000; ++it) {
      POLL_WAIT(p.a);
      if (__builtin_amdgcn_ballot_w64(active && p.a != want) == 0) break;
      POLL_ISSUE(p.a);
      POLL_WAIT(p.b);
      if (__builtin_amdgcn_ballot_w64(active && p.b != want) == 0) break;
      POLL_ISSUE(p.b);
      POLL_WAIT(p.c);
      if (__builtin_amdgcn_ballot_w64(active && p.c != want) == 0) break;
      POLL_ISSUE(p.c);
    }
  }
  if (active) __hip_atomic_store((gu64*)(ws + WS_E) + poll, want, RLX_AGENT);
}
DEVI void poll_retire(Polls& p) { asm volatile("s_waitcnt vmcnt(0)" : "+v"(p.a), "+v"(p.b), "+v"(p.c)::"memory"); }
DEVI void spin_slots(char* ws, uint32_t slot, u64 want, bool active) {
  const gu64* f = (const gu64*)(ws + WS_F) + slot;
#pragma unroll 1
  for (int it = 0; it < 200000; ++it) {
    const u64 v = active ? __hip_atomic_load(f, RLX_AGENT) : want;
    if (__builtin_amdgcn_ballot_w64(v != want) == 0) break;
    __builtin_amdgcn_s_sleep(1);
  }
}
DEVI void signal_slots(char* ws, uint32_t slot, u64 val, bool active) {
  if (active) __hip_atomic_store((gu64*)(ws + WS_F) + slot, val, RLX_AGENT);
}

constexpr uint32_t CODE_PF = 20992;
constexpr int STAGE_BYTES = 40960;
constexpr int L_WPHI = 3 * STAGE_BYTES;
constexpr int L_K = 0, L_V = 16384, L_ONES = 32768, L_RED = 33792, L_KVF = L_RED + 17408, L_WO = 57344;
constexpr int A_AT = 3;
constexpr int L_WPHI2 = L_WPHI + 2048;
constexpr int L_PF = L_WPHI2 + 1024;
constexpr int LDS_BYTES = L_PF + 256;

__global__ __launch_bounds__(512, 2) void fused(const u16* __restrict__ xA, const u16* __restrict__ wA,
                                                const u16* __restrict__ wphi, const u16* __restrict__ woA,
                                                const u16* __restrict__ xB, const u16* __restrict__ wB,
                                                const u16* __restrict__ woB, char* __restrict__ ws,
                                                u16* __restrict__ out) {
  __shared__ __attribute__((aligned(16))) char smem[LDS_BYTES];
  {
    const char* pc;
    asm volatile("s_getpc_b64 %0" : "=s"(pc));
    const uint32_t o = threadIdx.x * 64u;
    glds4s(pc, o < CODE_PF ? o : 0u, lds_addr_of(smem) + L_PF);
  }
  const int tid = threadIdx.x, lane = tid & 63;
  const int w = __builtin_amdgcn_readfirstlane(tid >> 6);
  const int bid = blockIdx.x, xcd = bid & 7, idx = bid >> 3;
  const int h = lane >> 5;
  const int br1 = xcd >> 2, jx = xcd & 3;
  const int ti1 = (jx & 1) * 8 + (idx & 7);
  const int g1 = (jx >> 1) * 4 + (idx >> 3);
  const int head = bid & 31, chunk = bid >> 5;
  const int br3 = xcd >> 2, ti3 = (xcd & 3) * 4 + (idx & 3), nj = idx >> 2;
  const __amdgpu_buffer_rsrc_t wsr = __builtin_amdgcn_make_buffer_rsrc(ws, 0, WS_END, 0x00020000);
  const uint32_t sm0 = lds_addr_of(smem);

  const uint32_t cslot = bid * 64 + lane;
  uint32_t pslot;
  bool pact;
  u64 cwant = 0, pval = 0;
  Polls polls = {0, 0, 0};

  {
    const u16* x = br1 ? xB : xA;
    const u16* W = br1 ? wB : wA;
    const int tt = w & 3, hp = w >> 2;
    const uint32_t xo0 = (uint32_t)((ti1 * 128 + 16 * w + (lane >> 3)) * DM + (((lane & 7) ^ ((lane >> 4) & 7)) * 8)) * 2u;
    const uint32_t xo1 = (uint32_t)((ti1 * 128 + 16 * w + 8 + (lane >> 3)) * DM + (((lane & 7) ^ ((4 + (lane >> 4)) & 7)) * 8)) * 2u;
    const uint32_t wo0 = (uint32_t)((8 * w + (lane >> 3)) * 1536 + g1 * 64 + (((lane & 7) ^ (((lane >> 4) & 1) << 2)) * 8)) * 2u;
    const int s0 = ((idx & 7) + 2 * (idx >> 3)) & 7;
    auto stage_load = [&](int su, int buf, int part) {
      const int s = (su + s0) & 7;
      const uint32_t b = sm0 + buf * STAGE_BYTES;
      if (part == 0) {
        glds16s(x + s * 64, xo0, b + w * 2048);
        glds16s(x + s * 64, xo1, b + w * 2048 + 1024);
      } else if (part == 1) {
        glds16s(W + (size_t)s * 64 * 1536, wo0, b + 16384 + w * 1024);
        glds16s(W + (size_t)s * 64 * 1536 + 512, wo0, b + 16384 + 8192 + w * 1024);
      } else {
        glds16s(W + (size_t)s * 64 * 1536 + 1024, wo0, b + 16384 + 16384 + w * 1024);
      }
    };
    asm volatile("" ::"s"(out), "s"(ws), "s"(woA), "s"(woB));
    for (int p = 0; p < 3; ++p) stage_load(0, 0, p);
    for (int p = 0; p < 3; ++p) stage_load(1, 1, p);
    if (w == 1) glds16s(wphi + (size_t)(head & ~1) * 256, (uint32_t)lane * 16u, sm0 + L_WPHI2);
    if (br1 == 0 && w >= 6) glds16s(wphi + (size_t)(4 * g1) * 256 + (w - 6) * 512, (uint32_t)lane * 16u, sm0 + L_WPHI + (w - 6) * 1024);
    {
      const int cc = (ti1 >> 1) + (lane >> 2);
      const int j = lane - 32, tj = 2 * chunk + ((j >> 3) & 1);
      const int c3 = ((j & 7) * 4 + (tj & 3)) * 8 + (j >> 4) * 4 + (tj >> 2);
      if (lane >= 32) { pact = true; pslot = c3 * 64 + 32 + head; }
      else if (br1) { pact = lane < 8 && cc <= 7; pslot = (cc * 32 + 4 * g1 + (lane & 3)) * 64 + (ti1 - 2 * cc + 2); }
      else { pact = true; pslot = ((lane >> 2) * 32 + 4 * g1 + (lane & 3)) * 64 + 16 + ti1; }
      if (!pact) pslot = cslot;
    }
    cwant = __hip_atomic_load((const gu64*)(ws + WS_E) + cslot, RLX_AGENT);
    pval = __hip_atomic_load((const gu64*)(ws + WS_E) + pslot, RLX_AGENT);

    const int xrow = 32 * tt + (lane & 31);
    const int xa0 = xrow * 128 + ((h ^ ((xrow >> 1) & 7)) * 16);
    const int q4 = (lane & 15) >> 2, p4 = lane & 3;
    const int wa0 = 16384 + (8 * h + q4) * 128 + (((4 * hp + 2 * ((lane >> 4) & 1) + (p4 >> 1)) ^ (((q4 >> 1) & 1) << 2)) * 16) + 8 * (p4 & 1);

    f32x16 aq = zero16(), ak = zero16(), av = zero16();
    bf16x8 xf[2], fq[2], fk[2], fv[2];
    auto frags = [&](const char* b, int ks, int p) {
      xf[p] = lds_read16(b + (xa0 ^ (32 * ks)));
      const char* wb = b + wa0 + ks * 2048;
      fq[p] = tr_frag(wb, wb + 512);
      fk[p] = tr_frag(wb + 8192, wb + 8192 + 512);
      fv[p] = tr_frag(wb + 16384, wb + 16384 + 512);
    };
    auto mma = [&](int p) {
      aq = MFMA32(fq[p], xf[p], aq);
      ak = MFMA32(fk[p], xf[p], ak);
      av = MFMA32(xf[p], fv[p], av);
    };
#pragma unroll
    for (int s = 0; s < 8; ++s) {
      if (s == 0) asm volatile("s_waitcnt vmcnt(7)" ::: "memory");
      else if (s < 7) asm volatile("s_waitcnt vmcnt(5)" ::: "memory");
      else asm volatile("s_waitcnt vmcnt(0)" ::: "memory");
      __builtin_amdgcn_s_barrier();
      asm volatile("" ::: "memory");
      const char* b = smem + (s % 3) * STAGE_BYTES;
      frags(b, 0, 0);
      if (s + 2 < 8) stage_load(s + 2, (s + 2) % 3, 0);
      __builtin_amdgcn_sched_barrier(0);
      if (s > 0) mma(1);
      __builtin_amdgcn_sched_barrier(0);
      frags(b, 1, 1);
      if (s + 2 < 8) stage_load(s + 2, (s + 2) % 3, 1);
      __builtin_amdgcn_sched_barrier(0);
      mma(0);
      __builtin_amdgcn_sched_barrier(0);
      frags(b, 2, 0);
      if (s + 2 < 8) stage_load(s + 2, (s + 2) % 3, 2);
      __builtin_amdgcn_sched_barrier(0);
      mma(1);
      __builtin_amdgcn_sched_barrier(0);
      frags(b, 3, 1);
      __builtin_amdgcn_sched_barrier(0);
      mma(0);
      __builtin_amdgcn_sched_barrier(0);
      asm volatile("s_waitcnt lgkmcnt(0)" ::: "memory");
    }
    mma(1);
    const int tile32 = ti1 * 4 + tt, head0 = 4 * g1 + 2 * hp, blk = (lane >> 4) & 1;
    if (br1) {
      const uint32_t qo = (head0 * NT + tile32) * 1024 + lane * 16;
      st_sc1_frag(wsr, WS_QB + qo, pack8s<0>(aq, 0.25f * L2E));
      st_sc1_frag(wsr, WS_QB + qo + NT * 1024, pack8s<8>(aq, 0.25f * L2E));
      st_sc1_frag(wsr, WS_KB + qo, pack8<0>(ak));
      st_sc1_frag(wsr, WS_KB + qo + NT * 1024, pack8<8>(ak));
      const uint32_t vo = WS_VB + ((head0 + ((lane >> 4) & 1)) * NT + tile32) * 1024 + h * 256 + (lane & 15) * 16;
      st_sc1_frag(wsr, vo, pack8<0>(av));
      st_sc1_frag(wsr, vo + 512, pack8<8>(av));
    } else {
      const uint32_t qo = WS_QA + (head0 * NT + tile32) * 1024 + lane * 16;
      st_sc1_frag(wsr, qo, pack8<0>(aq));
      st_sc1_frag(wsr, qo + NT * 1024, pack8<8>(aq));
      s16x8 i0, i1;
#pragma unroll
      for (int j = 0; j < 8; ++j) {
        const int f = 8 * (j >> 2) + 4 * h + (j & 3);
        i0[j] = ((lane & 31) == f) ? (short)0x3F80 : (short)0;
        i1[j] = ((lane & 31) == 16 + f) ? (short)0x3F80 : (short)0;
      }
      const bf16x8 id0 = __builtin_bit_cast(bf16x8, i0), id1 = __builtin_bit_cast(bf16x8, i1);
      const u32x4 ones = {0x3F803F80u, 0x3F803F80u, 0x3F803F80u, 0x3F803F80u};
      const u32x4 vf0 = __builtin_bit_cast(u32x4, pack8<0>(av)), vf1 = __builtin_bit_cast(u32x4, pack8<8>(av));
      f32x16 pk0 = MFMA32(wphi_frag_lds(smem + L_WPHI + (2 * hp) * 512, lane), pack8<0>(ak), zero16());
      f32x16 pk1 = MFMA32(wphi_frag_lds(smem + L_WPHI + (2 * hp + 1) * 512, lane), pack8<8>(ak), zero16());
      bf16x8 fp[2], fn[2];
      dual_softmax(pk0, fp[0], fn[0]);
      dual_softmax(pk1, fp[1], fn[1]);
#pragma unroll
      for (int p = 0; p < 2; ++p) {
        f32x16 T = MFMA32(fp[p], id0, zero16());
        T = MFMA32(fn[p], id1, T);
        const bool mine = blk == p;
        f32x16 kvp = MFMA32(pack8<0>(T), __builtin_bit_cast(bf16x8, mine ? vf0 : ones), zero16());
        kvp = MFMA32(pack8<8>(T), __builtin_bit_cast(bf16x8, mine ? vf1 : ones), kvp);
        if (mine || (lane & 15) == 0) {
          char* dst = smem + ((2 * hp + p) * 4 + tt) * 2176 + (mine ? (h * 16 + (lane & 15)) * 16 : 2048 + h * 16);
          const int st = mine ? 512 : 32;
#pragma unroll
          for (int r4 = 0; r4 < 4; ++r4) {
            f32x4 v = {kvp[4 * r4], kvp[4 * r4 + 1], kvp[4 * r4 + 2], kvp[4 * r4 + 3]};
            *(f32x4*)(dst + r4 * st) = v;
          }
        }
      }
      wg_barrier();
      if (w < 4 || (w == 4 && lane < 16)) {
        const int hh = w < 4 ? w : (lane >> 2);
        const char* src = smem + hh * 4 * 2176 + (w < 4 ? (lane >> 5) * 1024 + (lane & 31) * 16 : 2048 + (lane & 3) * 32);
        const int d2 = w < 4 ? 512 : 16;
        f32x4 s0v = *(const f32x4*)src, s1v = *(const f32x4*)(src + d2);
#pragma unroll
        for (int t = 1; t < 4; ++t) {
          s0v += *(const f32x4*)(src + t * 2176);
          s1v += *(const f32x4*)(src + t * 2176 + d2);
        }
        bf16x8 o;
#pragma unroll
        for (int c = 0; c < 4; ++c) { o[c] = (__bf16)s0v[c]; o[4 + c] = (__bf16)s1v[c]; }
        st_sc1_frag(wsr, WS_KVP + ((4 * g1 + hh) * 16 + ti1) * KVP_BYTES + (w < 4 ? lane * 16 : 1024 + (lane & 3) * 16), o);
      }
    }
    u64 first = 0;
    if (w == 0) first = poll_slot(ws, bid * 64 + (lane & 3));
    drain_barrier();
    cwant = mix(cwant);
    if (w == 0) {
      pval = mix(pval);
      signal_slots(ws, pslot, pval, pact && lane < 32);
      wait_slots(ws, bid * 64 + (lane & 3), cwant, lane < 4 && 2 * chunk - 2 + lane >= 0, first, polls);
    }
    if (tid >= 448) {
      const u32x4 ones = {0x3F803F80u, 0x3F803F80u, 0x3F803F80u, 0x3F803F80u};
      *(u32x4*)(smem + L_ONES + (tid - 448) * 16) = ones;
    }
    wg_barrier();
  }

  const int qt = chunk * 8 + w;
  const bool lower = (lane & 16) == 0;
  bf16x8 qf;
  {
    const int t0 = chunk * 8 - 8;
    u32x4 kv[4];
    if (t0 + 2 * w >= 0) {
      const uint32_t o = (head * NT + t0 + 2 * w) * 1024 + lane * 16;
      kv[0] = ld_sc1(wsr, WS_KB + o);
      kv[1] = ld_sc1(wsr, WS_KB + o + 1024);
      kv[2] = ld_sc1(wsr, WS_VB + o);
      kv[3] = ld_sc1(wsr, WS_VB + o + 1024);
    }
    qf = ld_sc1_frag(wsr, WS_QB + (head * NT + qt) * 1024 + lane * 16);
    if (t0 + 2 * w >= 0) {
      *(u32x4*)(smem + L_K + 2 * w * 1024 + lane * 16) = kv[0];
      *(u32x4*)(smem + L_K + 2 * w * 1024 + 1024 + lane * 16) = kv[1];
      *(u32x4*)(smem + L_V + 2 * w * 1024 + lane * 16) = kv[2];
      *(u32x4*)(smem + L_V + 2 * w * 1024 + 1024 + lane * 16) = kv[3];
    }
    poll_retire(polls);
    wg_barrier();
  }
  const bool aslot = lane >= 16 && lane < 32;
  const u64 early = __hip_atomic_load((const gu64*)(ws + WS_F) + cslot, RLX_AGENT);
  u32x4 pp[2], pz;
  bf16x8 qa;
  const u16* wo = br3 ? woB : woA;
  auto wo_load = [&]() {
    const uint32_t off = (uint32_t)((64 * w + (lane >> 3)) * DM + nj * 64 + (((lane & 7) ^ (((lane >> 4) & 1) << 2)) * 8)) * 2u;
#pragma unroll
    for (int i = 0; i < 8; ++i) glds16s(wo + i * 8 * DM, off, sm0 + L_WO + w * 8192 + i * 1024);
  };

  {
    const char* kbase = smem + L_K + w * 1024 + lane * 16;
    const int vinc = lower ? 1024 : 0;
    const char* vbase = lower ? (smem + L_V + w * 1024 + h * 256 + (lane & 15) * 16) : (smem + L_ONES + (lane & 15) * 16);
    const int ql = lane & 31;
    const int first = chunk > 0 ? 0 : 8 - w;
    auto a_fetch = [&]() {
      if (__builtin_amdgcn_ballot_w64(aslot && early != cwant) != 0) spin_slots(ws, cslot, cwant, aslot);
      asm volatile("" ::: "memory");
      if (w == 0 && aslot) __hip_atomic_store((gu64*)(ws + WS_E) + cslot, cwant, RLX_AGENT);
      const uint32_t kb = WS_KVP + (head * 16 + 2 * w) * KVP_BYTES;
      pp[0] = ld_sc1(wsr, kb + lane * 16);
      pp[1] = ld_sc1(wsr, kb + KVP_BYTES + lane * 16);
      pz = ld_sc1(wsr, kb + ((lane >> 2) & 1) * KVP_BYTES + 1024 + (lane & 3) * 16);
      qa = ld_sc1_frag(wsr, WS_QA + (head * NT + qt) * 1024 + lane * 16);
    };
    auto a_readout = [&]() {
      f32x4 s0v, s1v, z0, z1;
      s0v[0] = __uint_as_float(pp[0][0] << 16) + __uint_as_float(pp[1][0] << 16);
      s0v[1] = __uint_as_float(pp[0][0] & 0xFFFF0000u) + __uint_as_float(pp[1][0] & 0xFFFF0000u);
      s0v[2] = __uint_as_float(pp[0][1] << 16) + __uint_as_float(pp[1][1] << 16);
      s0v[3] = __uint_as_float(pp[0][1] & 0xFFFF0000u) + __uint_as_float(pp[1][1] & 0xFFFF0000u);
      s1v[0] = __uint_as_float(pp[0][2] << 16) + __uint_as_float(pp[1][2] << 16);
      s1v[1] = __uint_as_float(pp[0][2] & 0xFFFF0000u) + __uint_as_float(pp[1][2] & 0xFFFF0000u);
      s1v[2] = __uint_as_float(pp[0][3] << 16) + __uint_as_float(pp[1][3] << 16);
      s1v[3] = __uint_as_float(pp[0][3] & 0xFFFF0000u) + __uint_as_float(pp[1][3] & 0xFFFF0000u);
      z0[0] = __uint_as_float(pz[0] << 16); z0[1] = __uint_as_float(pz[0] & 0xFFFF0000u);
      z0[2] = __uint_as_float(pz[1] << 16); z0[3] = __uint_as_float(pz[1] & 0xFFFF0000u);
      z1[0] = __uint_as_float(pz[2] << 16); z1[1] = __uint_as_float(pz[2] & 0xFFFF0000u);
      z1[2] = __uint_as_float(pz[3] << 16); z1[3] = __uint_as_float(pz[3] & 0xFFFF0000u);
#pragma unroll
      for (int c = 0; c < 4; ++c) { z0[c] += __shfl_xor(z0[c], 4); z1[c] += __shfl_xor(z1[c], 4); }
      char* red = smem + L_RED;
      *(f32x4*)(red + w * 2048 + lane * 16) = s0v;
      *(f32x4*)(red + w * 2048 + 1024 + lane * 16) = s1v;
      if (lane < 4) {
        *(f32x4*)(red + 16384 + w * 128 + lane * 32) = z0;
        *(f32x4*)(red + 16384 + w * 128 + lane * 32 + 16) = z1;
      }
      wo_load();
      const bf16x8 wf = wphi_frag_lds(smem + L_WPHI2 + (head & 1) * 512, lane);
      const f32x16 pq = MFMA32(wf, qa, zero16());
      wg_barrier();
      if (w == 0) {
        const char* src = red + lane * 16;
        f32x4 t0 = *(const f32x4*)src, t1 = *(const f32x4*)(src + 1024);
#pragma unroll
        for (int t = 1; t < 8; ++t) { t0 += *(const f32x4*)(src + t * 2048); t1 += *(const f32x4*)(src + t * 2048 + 1024); }
        bf16x8 o;
#pragma unroll
        for (int c = 0; c < 4; ++c) { o[c] = (__bf16)t0[c]; o[4 + c] = (__bf16)t1[c]; }
        *(bf16x8*)(smem + L_KVF + (lane >> 5) * 1024 + (((lane >> 4) & 1) * 32 + (lane & 15)) * 16) = o;
      }
      if (w == 1) {
        const int f = lane & 31;
        const float* zr = (const float*)(red + 16384);
        const __bf16 z = (__bf16)(((zr[f] + zr[32 + f]) + (zr[64 + f] + zr[96 + f])) + ((zr[128 + f] + zr[160 + f]) + (zr[192 + f] + zr[224 + f])));
#pragma unroll
        for (int i = 0; i < 8; ++i)
          *(__bf16*)(smem + L_KVF + (f >> 4) * 1024 + (((f >> 2) & 1) * 32 + 16 + 8 * h + i) * 16 + (((f >> 3) & 1) * 4 + (f & 3)) * 2) = z;
      }
      bf16x8 fp, fn;
      dual_softmax(pq, fp, fn);
      wg_barrier();
      f32x16 oa = MFMA32(lds_read16(smem + L_KVF + lane * 16), fp, zero16());
      oa = MFMA32(lds_read16(smem + L_KVF + 1024 + lane * 16), fn, oa);
      const float inv = __builtin_amdgcn_rcpf(oa[8] + 1e-6f);
      st_sc1_frag(wsr, WS_O + (head * NT + qt) * 1024 + lane * 16, pack8s<0>(oa, inv));
    };
    auto boundary_p = [&](const f32x16& sm, float m, u32x4& da, u32x4& db, u32x4& ua, u32x4& ub) {
      f32x16 e;
#pragma unroll
      for (int r = 0; r < 16; ++r) e[r] = __builtin_amdgcn_exp2f(sm[r] - m);
      const u32x4 pa = __builtin_bit_cast(u32x4, pack8<0>(e)), pb = __builtin_bit_cast(u32x4, pack8<8>(e));
#pragma unroll
      for (int i = 0; i < 4; ++i) {
        const int ra = 2 * i, rb = 8 + 2 * i;
        const int ka = (ra & 3) + 8 * (ra >> 2) + 4 * h, kb = (rb & 3) + 8 * (rb >> 2) + 4 * h;
        const unsigned ma = (ka <= ql ? 0xFFFFu : 0u) | (ka + 1 <= ql ? 0xFFFF0000u : 0u);
        const unsigned mb = (kb <= ql ? 0xFFFFu : 0u) | (kb + 1 <= ql ? 0xFFFF0000u : 0u);
        da[i] = pa[i] & ma; ua[i] = pa[i] & ~ma;
        db[i] = pb[i] & mb; ub[i] = pb[i] & ~mb;
      }
    };
    f32x16 o = zero16();
    if (chunk > 0) {
      bf16x8 kf[9];
      kf[8] = lds_read16(kbase + 8 * 1024);
      kf[0] = lds_read16(kbase);
      kf[1] = lds_read16(kbase + 1024);
      kf[2] = lds_read16(kbase + 2048);
      f32x16 sm = MFMA32(kf[8], qf, zero16());
      f32x16 s0 = MFMA32(kf[0], qf, zero16());
      f32x16 sc = MFMA32(kf[1], qf, zero16());
#pragma unroll
      for (int r = 0; r < 16; ++r) {
        const int kl = (r & 3) + 8 * (r >> 2) + 4 * h;
        sm[r] = (kl <= ql) ? sm[r] : s0[r];
      }
      float m = sm[0];
#pragma unroll
      for (int r = 1; r < 16; ++r) m = fmaxf(m, sm[r]);
#pragma unroll
      for (int i = 1; i < 8; ++i) {
        f32x16 sn;
        if (i + 2 < 8) kf[i + 2] = lds_read16(kbase + (i + 2) * 1024);
        if (i < 7) sn = MFMA32(kf[i + 1], qf, zero16());
        __builtin_amdgcn_sched_barrier(0);
#pragma unroll
        for (int r = 0; r < 16; r += 2) m = fmaxf(fmaxf(m, sc[r]), sc[r + 1]);
        __builtin_amdgcn_sched_barrier(0);
        if (i < 7) sc = sn;
      }
      m = swap_max(m);
      a_fetch();
      f32x16 negm;
#pragma unroll
      for (int r = 0; r < 16; ++r) negm[r] = -m;
      sc = MFMA32(kf[1], qf, negm);
      {
        const bf16x8 vd0 = lds_read16(vbase + 8 * vinc), vd1 = lds_read16(vbase + 8 * vinc + 512);
        const bf16x8 vu0 = lds_read16(vbase), vu1 = lds_read16(vbase + 512);
        u32x4 da, db, ua, ub;
        boundary_p(sm, m, da, db, ua, ub);
        o = MFMA32(vd0, __builtin_bit_cast(bf16x8, da), o);
        o = MFMA32(vd1, __builtin_bit_cast(bf16x8, db), o);
        o = MFMA32(vu0, __builtin_bit_cast(bf16x8, ua), o);
        o = MFMA32(vu1, __builtin_bit_cast(bf16x8, ub), o);
      }
#pragma unroll
      for (int i = 1; i < 8; ++i) {
        const bf16x8 v0 = lds_read16(vbase + i * vinc), v1 = lds_read16(vbase + i * vinc + 512);
        f32x16 sn, e;
        __builtin_amdgcn_sched_barrier(0);
#pragma unroll
        for (int r = 0; r < 4; ++r) e[r] = __builtin_amdgcn_exp2f(sc[r]);
        __builtin_amdgcn_sched_barrier(0);
        if (i < 7) sn = MFMA32(kf[i + 1], qf, negm);
        __builtin_amdgcn_sched_barrier(0);
#pragma unroll
        for (int r = 4; r < 8; ++r) e[r] = __builtin_amdgcn_exp2f(sc[r]);
        const bf16x8 p0 = pack8<0>(e);
#pragma unroll
        for (int r = 8; r < 10; ++r) e[r] = __builtin_amdgcn_exp2f(sc[r]);
        __builtin_amdgcn_sched_barrier(0);
        o = MFMA32(v0, p0, o);
        __builtin_amdgcn_sched_barrier(0);
#pragma unroll
        for (int r = 10; r < 16; ++r) e[r] = __builtin_amdgcn_exp2f(sc[r]);
        const bf16x8 p1 = pack8<8>(e);
        __builtin_amdgcn_sched_barrier(0);
        o = MFMA32(v1, p1, o);
        __builtin_amdgcn_sched_barrier(0);
        if (i < 7) sc = sn;
        if (i == A_AT) { a_readout(); __builtin_amdgcn_sched_barrier(0); }
      }
    } else {
      f32x16 sm = MFMA32(lds_read16(kbase + 8 * 1024), qf, zero16());
#pragma unroll
      for (int r = 0; r < 16; ++r) {
        const int kl = (r & 3) + 8 * (r >> 2) + 4 * h;
        sm[r] = (kl <= ql) ? sm[r] : -INFINITY;
      }
      float m = sm[0];
#pragma unroll
      for (int r = 1; r < 16; ++r) m = fmaxf(m, sm[r]);
#pragma unroll 1
      for (int i = first; i < 8; ++i) {
        const f32x16 s = MFMA32(lds_read16(kbase + i * 1024), qf, zero16());
#pragma unroll
        for (int r = 0; r < 16; r += 2) m = fmaxf(fmaxf(m, s[r]), s[r + 1]);
      }
      m = swap_max(m);
      a_fetch();
      u32x4 da, db, ua, ub;
      boundary_p(sm, m, da, db, ua, ub);
      o = MFMA32(lds_read16(vbase + 8 * vinc), __builtin_bit_cast(bf16x8, da), o);
      o = MFMA32(lds_read16(vbase + 8 * vinc + 512), __builtin_bit_cast(bf16x8, db), o);
#pragma unroll 1
      for (int i = first; i < 8; ++i) {
        const f32x16 s = MFMA32(lds_read16(kbase + i * 1024), qf, zero16());
        f32x16 e;
#pragma unroll
        for (int r = 0; r < 16; ++r) e[r] = __builtin_amdgcn_exp2f(s[r] - m);
        o = MFMA32(lds_read16(vbase + i * vinc), pack8<0>(e), o);
        o = MFMA32(lds_read16(vbase + i * vinc + 512), pack8<8>(e), o);
      }
      a_readout();
    }
    const float inv = __builtin_amdgcn_rcpf(o[8]);
    st_sc1_frag(wsr, WS_O + FRAG_BYTES + (head * NT + qt) * 1024 + lane * 16, pack8s<0>(o, inv));
    drain_barrier();
    if (w == 0) {
      signal_slots(ws, pslot, pval, lane >= 32);
      wait_slots(ws, cslot | 32, cwant, lane >= 32, ~cwant, polls);
    }
    wg_barrier();
  }

  {
    const int tt = w & 3, kh = w >> 2;
    const int tile32 = ti3 * 4 + tt;
    const uint32_t og = WS_O + br3 * FRAG_BYTES + ((16 * kh) * NT + tile32) * 1024 + lane * 16;
    bf16x8 of[16];
#pragma unroll
    for (int i = 0; i < 16; ++i) of[i] = ld_sc1_frag(wsr, og + i * NT * 1024);
    __builtin_amdgcn_sched_barrier(0);
    const int blk = (lane >> 4) & 1, q4 = (lane & 15) >> 2, p4 = lane & 3;
    f32x16 acc0 = zero16(), acc1 = zero16();
    const char* wl = smem + L_WO;
    const int ra0 = (256 * kh + 4 * h + q4) * 128 + (((2 * blk + (p4 >> 1)) ^ (((q4 >> 1) & 1) << 2)) * 16) + 8 * (p4 & 1);
    const int ra1 = (256 * kh + 4 * h + q4) * 128 + (((4 + 2 * blk + (p4 >> 1)) ^ (((q4 >> 1) & 1) << 2)) * 16) + 8 * (p4 & 1);
#pragma unroll
    for (int i = 0; i < 16; ++i) {
      const int kb = 16 * i * 128;
      const bf16x8 a0 = tr_frag(wl + ra0 + kb, wl + ra0 + kb + 1024);
      const bf16x8 a1 = tr_frag(wl + ra1 + kb, wl + ra1 + kb + 1024);
      acc0 = MFMA32(a0, of[i], acc0);
      acc1 = MFMA32(a1, of[i], acc1);
    }
    poll_retire(polls);
    float* ex = (float*)smem + w * 1024 + lane * 4;
    const f32x16 give = kh ? acc0 : acc1;
    f32x16 keep = kh ? acc1 : acc0;
#pragma unroll
    for (int r4 = 0; r4 < 4; ++r4) {
      f32x4 v = {give[4 * r4], give[4 * r4 + 1], give[4 * r4 + 2], give[4 * r4 + 3]};
      *(f32x4*)(ex + r4 * 256) = v;
    }
    wg_barrier();
    const float* px = (const float*)smem + (w ^ 4) * 1024 + lane * 4;
#pragma unroll
    for (int r4 = 0; r4 < 4; ++r4) {
      const f32x4 v = *(const f32x4*)(px + r4 * 256);
#pragma unroll
      for (int c = 0; c < 4; ++c) keep[4 * r4 + c] += v[c];
    }
    const u32x4 lo = __builtin_bit_cast(u32x4, pack8<0>(keep)), hi = __builtin_bit_cast(u32x4, pack8<8>(keep));
    u16* op = out + (size_t)(tile32 * 32 + (lane & 31)) * 1024 + br3 * 512 + nj * 64 + kh * 32 + h * 8;
    auto t0 = __builtin_amdgcn_permlane32_swap(lo[0], lo[2], false, false);
    auto t1 = __builtin_amdgcn_permlane32_swap(lo[1], lo[3], false, false);
    u32x4 v = {t0[0], t1[0], t0[1], t1[1]};
    *(u32x4*)op = v;
    auto t2 = __builtin_amdgcn_permlane32_swap(hi[0], hi[2], false, false);
    auto t3 = __builtin_amdgcn_permlane32_swap(hi[1], hi[3], false, false);
    u32x4 u = {t2[0], t3[0], t2[1], t3[1]};
    *(u32x4*)(op + 16) = u;
  }
}

extern "C" void kernel_launch(void* const* d_in, const int* in_sizes, int n_in, void* d_out, int out_size,
                              void* d_ws, size_t ws_size, hipStream_t stream) {
  fused<<<256, 512, 0, stream>>>((const u16*)d_in[0], (const u16*)d_in[1], (const u16*)d_in[2], (const u16*)d_in[3],
                                 (const u16*)d_in[4], (const u16*)d_in[5], (const u16*)d_in[6], (char*)d_ws, (u16*)d_out);
}
```
